```python
import torch

def _softmax(x, axis=-1):
    return torch.softmax(x, dim=axis)
def _gelu(x):
    return torch.nn.functional.gelu(x, approximate="tanh")
def _silu(x):
    return torch.nn.functional.silu(x)
def _sigmoid(x):
    return torch.sigmoid(x)
def _layernorm(x, axis=-1, eps=1e-5):
    m = x.mean(dim=axis, keepdim=True)
    v = x.var(dim=axis, keepdim=True, unbiased=False)
    return (x - m) / torch.sqrt(v + eps)
def _scatter_add(tgt, idx, src, axis=0):
    return tgt.clone().index_add_(axis, idx, src)
def _segment_sum(src, seg, n):
    out = torch.zeros((n,) + src.shape[1:], dtype=src.dtype)
    return out.index_add_(0, seg, src)
def _pad(x, pw):
    flat = tuple(v for pair in reversed(pw) for v in pair)
    return torch.nn.functional.pad(x, flat)

kernel_name = 'interleave_layers__sliding_block_cached__mixer_channel_mlp__decode'

def setup_inputs():

    _w = (lambda a: (lambda t: t.to(torch.bfloat16) if t.is_floating_point() else t)(torch.as_tensor(a)))
    def _A_setup():
        B, Sq, D = 4, 1, 512
        Hq, Hkv, Sk, Dh = 32, 8, 8192, 128
        assert Hq % Hkv == 0, (Hq, Hkv)
        return {
            "x": _w((lambda *s: torch.randn(*s, dtype=torch.bfloat16) * 0.05)(B, Sq, D)),
            "k_cache": _w((lambda *s: torch.randn(*s, dtype=torch.bfloat16) * 1.6)(B, Hkv, Sk, Dh)),
            "v_cache": _w((lambda *s: torch.randn(*s, dtype=torch.bfloat16) * 1.6)(B, Hkv, Sk, Dh)),
            "wq": _w((lambda *s: torch.randn(*s, dtype=torch.bfloat16) * 1.6)(D, Hq * Dh) * (D ** -0.5)),
            "wo": _w((lambda *s: torch.randn(*s, dtype=torch.bfloat16) * 1.6)(Hq * Dh, D) * ((Hq * Dh) ** -0.5)),
        }
    _A_in = _A_setup()

    def _B_setup():
        B, S, D, F = 4, 1, 512, 1792
        return {
            "x": _w((lambda *s: torch.randn(*s, dtype=torch.bfloat16) * 0.05)(B, S, D)),
            "w1": _w((lambda *s: torch.randn(*s, dtype=torch.bfloat16) * 3.2)(D, F) * (D ** -0.5)),
            "b1": _w((lambda *s: torch.randn(*s, dtype=torch.bfloat16) * 1.6)(F)),
            "w2": _w((lambda *s: torch.randn(*s, dtype=torch.bfloat16) * 0.2)(F, D) * (F ** -0.5)),
            "b2": _w((lambda *s: torch.randn(*s, dtype=torch.bfloat16) * 0.2)(D)),
        }
    _B_in = _B_setup()

    return {
        "A_x": _A_in["x"],
        "A_k_cache": _A_in["k_cache"],
        "A_v_cache": _A_in["v_cache"],
        "A_wq": _A_in["wq"],
        "A_wo": _A_in["wo"],
        "B_x": _B_in["x"],
        "B_w1": _B_in["w1"],
        "B_b1": _B_in["b1"],
        "B_w2": _B_in["w2"],
        "B_b2": _B_in["b2"],
    }

def reference(A_x, A_k_cache, A_v_cache, A_wq, A_wo, B_x, B_w1, B_b1, B_w2, B_b2):
    def _A_ref(x, k_cache, v_cache, wq, wo):
        B, Sq, D = x.shape
        Hq, Dh = 32, 128
        Hkv, Sk = k_cache.shape[1], k_cache.shape[2]
        W = min(4096, max(256, Sk // 4))
        rep = Hq // Hkv
        q = (lambda x, *d: x.permute(*d))((x @ wq).reshape(B, Sq, Hq, Dh), 0, 2, 1, 3)
        k = (lambda x, n, axis: torch.repeat_interleave(x, n, dim=axis))(k_cache[:, :, Sk - W:, :], rep, axis=1)
        v = (lambda x, n, axis: torch.repeat_interleave(x, n, dim=axis))(v_cache[:, :, Sk - W:, :], rep, axis=1)
        scores = torch.einsum("bhqd,bhkd->bhqk", q, k) * (Dh ** -0.5)
        p = _softmax(scores, axis=-1)
        o = torch.einsum("bhqk,bhkd->bhqd", p, v)
        return (lambda x, *d: x.permute(*d))(o, 0, 2, 1, 3).reshape(B, Sq, Hq * Dh) @ wo

    def _B_ref(x, w1, b1, w2, b2):
        y = _layernorm(x, axis=-1)
        h = _gelu(torch.einsum("bsd,df->bsf", y, w1) + b1)
        return torch.einsum("bsf,fd->bsd", h, w2) + b2

    x = A_x
    x = x + _A_ref(_layernorm(x, axis=-1), A_k_cache, A_v_cache, A_wq, A_wo)
    x = x + _B_ref(_layernorm(x, axis=-1), B_w1, B_b1, B_w2, B_b2)
    x = x + _A_ref(_layernorm(x, axis=-1), A_k_cache, A_v_cache, A_wq, A_wo)
    x = x + _B_ref(_layernorm(x, axis=-1), B_w1, B_b1, B_w2, B_b2)
    return x
```

```cpp
#include <hip/hip_runtime.h>
#include <hip/hip_bf16.h>
#include <stdint.h>

#define DIAG 0

typedef unsigned int u32;
typedef __attribute__((ext_vector_type(4))) unsigned int u32x4;
typedef __attribute__((ext_vector_type(2))) unsigned int u32x2;
typedef __attribute__((ext_vector_type(4))) float f32x4;
typedef __attribute__((ext_vector_type(2))) float f32x2;
typedef __attribute__((ext_vector_type(8))) __bf16 bf16x8;
typedef __attribute__((ext_vector_type(2))) __bf16 bf16x2;

namespace {
constexpr int SK = 8192, WIN = 2048, FF = 1792;
constexpr float EPS = 1e-5f;
constexpr u32 SPIN_CAP = 1u << 17;
constexpr int EP_OFF = 0;
constexpr int QP_OFF = 4096;
constexpr int CS_OFF = QP_OFF + 2 * 256 * 2048;
constexpr int X2_OFF = CS_OFF + 256 * 2048;
constexpr int OP_OFF = X2_OFF + 32 * 2048;
constexpr int ML_OFF = OP_OFF + 2 * 256 * 2048;
constexpr int WP_OFF = ML_OFF + 2 * 32 * 256;
constexpr int MP_OFF = WP_OFF + 2 * 4 * 16384;
constexpr int WS_END = MP_OFF + 2 * 4 * 8 * 56 * 256;
constexpr int DG_OFF = WS_END;
constexpr int AUX_SC1 = 16;

__device__ __forceinline__ float asf(u32 x) { return __builtin_bit_cast(float, x); }
__device__ __forceinline__ u32 asu(float x) { return __builtin_bit_cast(u32, x); }
__device__ __forceinline__ u32 pk_bf16(float lo, float hi) {
  f32x2 v = {lo, hi};
  return __builtin_bit_cast(u32, __builtin_convertvector(v, bf16x2));
}
__device__ __forceinline__ float rbf(float x) {
  return asf(pk_bf16(x, x) & 0xffff0000u);
}
template <int CTRL> __device__ __forceinline__ float dpp(float x) {
  return asf(__builtin_amdgcn_update_dpp(0, asu(x), CTRL, 0xf, 0xf, true));
}
constexpr int ROR8 = 0x128, ROR4 = 0x124, ROR2 = 0x122, ROR1 = 0x121, QX1 = 0xB1, QX2 = 0x4E;
__device__ __forceinline__ float rs32(float a, float b) {
  auto r = __builtin_amdgcn_permlane32_swap(asu(a), asu(b), false, false);
  return asf(r[0]) + asf(r[1]);
}
__device__ __forceinline__ float rs16(float a, float b) {
  auto r = __builtin_amdgcn_permlane16_swap(asu(a), asu(b), false, false);
  return asf(r[0]) + asf(r[1]);
}
__device__ __forceinline__ float rs8(float a, float b, bool bit3) {
  return (bit3 ? b : a) + dpp<ROR8>(bit3 ? a : b);
}
__device__ __forceinline__ float mx32(float a) {
  auto r = __builtin_amdgcn_permlane32_swap(asu(a), asu(a), false, false);
  return fmaxf(asf(r[0]), asf(r[1]));
}
__device__ __forceinline__ float mx16(float a) {
  auto r = __builtin_amdgcn_permlane16_swap(asu(a), asu(a), false, false);
  return fmaxf(asf(r[0]), asf(r[1]));
}
__device__ __forceinline__ float wave_sum(float a) {
  a += dpp<ROR8>(a); a += dpp<ROR4>(a); a += dpp<ROR2>(a); a += dpp<ROR1>(a);
  a = rs16(a, a); return rs32(a, a);
}
__device__ __forceinline__ f32x4 mfma(u32x4 a, u32x4 b, f32x4 c) {
  return __builtin_amdgcn_mfma_f32_16x16x32_bf16(__builtin_bit_cast(bf16x8, a), __builtin_bit_cast(bf16x8, b), c, 0, 0, 0);
}
template <int NW, typename V> __device__ __forceinline__ void tr8(const V (&r)[8], u32x4 (&t)[2 * NW]) {
#pragma unroll
  for (int c = 0; c < NW; ++c)
#pragma unroll
    for (int j = 0; j < 4; ++j) {
      t[2 * c][j] = __builtin_amdgcn_perm(r[2 * j + 1][c], r[2 * j][c], 0x05040100u);
      t[2 * c + 1][j] = __builtin_amdgcn_perm(r[2 * j + 1][c], r[2 * j][c], 0x07060302u);
    }
}
__device__ __forceinline__ u32x4 ldsc1(__amdgpu_buffer_rsrc_t rs, int off) {
  return __builtin_amdgcn_raw_buffer_load_b128(rs, off, 0, AUX_SC1);
}
__device__ __forceinline__ void stsc1(__amdgpu_buffer_rsrc_t rs, int off, u32x4 v) {
  __builtin_amdgcn_raw_buffer_store_b128(v, rs, off, 0, AUX_SC1);
}
__device__ __forceinline__ u32 tag8(float v, u32 t8) { return (asu(v) & 0xffffff00u) | t8; }
__device__ __forceinline__ u32 or4(u32x4 v) { return (v[0] | v[1]) | (v[2] | v[3]); }
__device__ __forceinline__ void wg_barrier() {
  asm volatile("s_waitcnt lgkmcnt(0)\n\ts_barrier" ::: "memory");
}
template <int N> __device__ __forceinline__ void gather(__amdgpu_buffer_rsrc_t rs, const int (&off)[N], const u32 (&tg)[N],
                                                        const u32 (&msk)[N], u32x4 (&v)[N]) {
  for (u32 spins = 0;; ++spins) {
    u32 bad = 0;
#pragma unroll
    for (int k = 0; k < N; ++k) v[k] = ldsc1(rs, off[k]);
#pragma unroll
    for (int k = 0; k < N; ++k) { v[k] ^= tg[k]; bad |= or4(v[k]) & msk[k]; }
    if (__all(bad == 0) || spins > SPIN_CAP) break;
    asm volatile("" ::: "memory");
  }
}
}

#define STAMP(i) do {} while (0)

__global__ void __launch_bounds__(512, 2)
fused_decode(const uint16_t* __restrict__ x, const uint16_t* __restrict__ kc, const uint16_t* __restrict__ vc,
             const uint16_t* __restrict__ wq, const uint16_t* __restrict__ wo, const uint16_t* __restrict__ w1,
             const uint16_t* __restrict__ b1, const uint16_t* __restrict__ w2, const uint16_t* __restrict__ b2,
             uint16_t* __restrict__ out, char* ws) {
  __shared__ __attribute__((aligned(16))) float s_ow[8 * 512];
  __shared__ __attribute__((aligned(16))) float s_ml[8 * 4 * 2];
  __shared__ __attribute__((aligned(16))) uint16_t s_q[512];
  __shared__ __attribute__((aligned(16))) float s_xa[512];
  __shared__ __attribute__((aligned(16))) float s_xb[512];
  __shared__ __attribute__((aligned(16))) float s_hp[16 * 32];
  __shared__ __attribute__((aligned(16))) float s_st[16];
  __shared__ __attribute__((aligned(16))) uint16_t s_hb[8 * 32];
  __shared__ __attribute__((aligned(16))) float s_xp[8 * 64];
  __shared__ __attribute__((aligned(16))) float s_wp[16 * 64];

  const int tid = threadIdx.x, bid = blockIdx.x;
  const int w = __builtin_amdgcn_readfirstlane(tid >> 6);
  const int l = tid & 63, i16 = l & 15, kg = l >> 4;
  const int g = bid & 7, s = (bid >> 3) & 7, b = bid >> 6;
  const int bg = b * 8 + g;
  const bool bit3 = (l & 8) != 0;
  const bool mlp_on = s < 7;
  const int f0 = g * 224 + (mlp_on ? s : 6) * 32;
  const __amdgpu_buffer_rsrc_t rs = __builtin_amdgcn_make_buffer_rsrc((void*)ws, 0, WS_END, 0x00020000);
  STAMP(0);

  const u32 ep = __builtin_amdgcn_raw_buffer_load_b32(rs, EP_OFF + b * 256, 0, AUX_SC1);
  const u32x4 xfull = *(const u32x4*)(x + b * 512 + l * 8);
  const u32x4 xa0 = *(const u32x4*)(x + b * 512 + s * 64 + (i16 & 1) * 32 + kg * 8);
  u32x4 wqr[8], kr[2][4], vr[8], wor[8];
  u32x2 w1r[8], w2r[8];
  {
    const uint16_t* p = wq + (size_t)(s * 64 + ((l >> 3) & 1) * 32 + kg * 8) * 4096 + g * 512 + w * 64 + (l & 7) * 8;
#pragma unroll
    for (int r = 0; r < 8; ++r) wqr[r] = *(const u32x4*)(p + r * 4096);
  }
  const size_t kvbase = ((size_t)bg * SK + (SK - WIN) + s * 256 + w * 32) * 128;
  {
    const uint16_t* p = kc + kvbase + ((i16 >> 2) * 8 + (i16 & 3)) * 128 + kg * 8;
#pragma unroll
    for (int t = 0; t < 2; ++t)
#pragma unroll
      for (int kk = 0; kk < 4; ++kk) kr[t][kk] = __builtin_nontemporal_load((const u32x4*)(p + t * 512 + kk * 32));
  }
  __builtin_amdgcn_sched_barrier(0);
  const u32 tn = __builtin_amdgcn_readfirstlane(ep) + 1u;
  const u32 t8 = tn & 0xffu, t16 = tn & 0xffffu;

  float mean, rstd;
  {
    float xv[8];
#pragma unroll
    for (int j = 0; j < 4; ++j) { xv[2 * j] = asf(xfull[j] << 16); xv[2 * j + 1] = asf(xfull[j] & 0xffff0000u); }
    float sm = 0.f;
#pragma unroll
    for (int j = 0; j < 8; ++j) sm += xv[j];
    mean = wave_sum(sm) * (1.f / 512.f);
    float sq = 0.f;
#pragma unroll
    for (int j = 0; j < 8; ++j) sq += (xv[j] - mean) * (xv[j] - mean);
    rstd = __builtin_amdgcn_rsqf(wave_sum(sq) * (1.f / 512.f) + EPS);
    if (w == 0) {
      *(f32x4*)(s_xa + l * 8) = f32x4{xv[0], xv[1], xv[2], xv[3]};
      *(f32x4*)(s_xa + l * 8 + 4) = f32x4{xv[4], xv[5], xv[6], xv[7]};
    }
  }
  u32x4 wqt[8];
  tr8<4>(wqr, wqt);
  const u32x4 ones = {0x3f803f80u, 0x3f803f80u, 0x3f803f80u, 0x3f803f80u};
  const f32x4 zero4 = {0.f, 0.f, 0.f, 0.f};

  auto s1_core = [&](u32x4 a, int dst) {
    f32x4 d[8];
#pragma unroll
    for (int c = 0; c < 8; ++c) d[c] = mfma(a, wqt[c], zero4);
    u32x4 o;
#pragma unroll
    for (int j = 0; j < 4; ++j) {
      const float keep = bit3 ? d[4 + j][1] : d[j][0];
      const float send = bit3 ? d[j][1] : d[4 + j][0];
      o[j] = tag8(keep + dpp<ROR8>(send), t8);
    }
    if (l < 16) stsc1(rs, dst + (w * 64 + (l & 7) * 8 + (l >> 3) * 4) * 4, o);
  };
  s1_core(xa0, QP_OFF + (bg * 8 + s) * 2048);
  s1_core(ones, CS_OFF + (bg * 8 + s) * 2048);
  STAMP(1);
  __builtin_amdgcn_sched_barrier(0);
  {
    const uint16_t* p = vc + kvbase + kg * 8 * 128 + i16 * 8;
#pragma unroll
    for (int r = 0; r < 8; ++r) vr[r] = __builtin_nontemporal_load((const u32x4*)(p + r * 128));
  }
  {
    const uint16_t* p = wo + (size_t)(g * 512 + w * 64 + ((l >> 3) & 1) * 32 + kg * 8) * 512 + s * 64 + (l & 7) * 8;
#pragma unroll
    for (int r = 0; r < 8; ++r) wor[r] = *(const u32x4*)(p + r * 512);
  }
  {
    const uint16_t* p = w1 + (size_t)(w * 64 + ((l >> 3) & 1) * 32 + kg * 8) * FF + f0 + (l & 7) * 4;
#pragma unroll
    for (int r = 0; r < 8; ++r) w1r[r] = *(const u32x2*)(p + r * FF);
    const uint16_t* p2 = w2 + (size_t)(f0 + kg * 8) * 512 + w * 64 + i16 * 4;
#pragma unroll
    for (int r = 0; r < 8; ++r) w2r[r] = *(const u32x2*)(p2 + r * 512);
  }
  const float b1v = asf((u32)b1[f0 + (l & 31)] << 16);
  const u32x4 b2a = *(const u32x4*)(b2 + s * 64 + (i16 & 1) * 32 + kg * 8);
  const float b2f = asf((u32)b2[s * 64 + g * 8 + ((l >> 5) * 4 + ((l >> 4) & 1) * 2 + ((l >> 3) & 1))] << 16);
  __builtin_amdgcn_sched_barrier(0);

  u32x4 vt[8], wot[8], w1t[4], w2t[4];
  float csq = 0.f, cs1 = 0.f;
  const int e8 = (l >> 5) * 4 + ((l >> 4) & 1) * 2 + ((l >> 3) & 1);
  auto red8 = [&](const float (&v)[8]) {
    const float u0 = rs32(v[0], v[4]), u1 = rs32(v[1], v[5]), u2 = rs32(v[2], v[6]), u3 = rs32(v[3], v[7]);
    return rs8(rs16(u0, u2), rs16(u1, u3), bit3);
  };

#pragma unroll
  for (int layer = 0; layer < 2; ++layer) {
    {
      const int qo = (bg * 8 + (l >> 3)) * 2048 + (w * 64 + (l & 7) * 8) * 4;
      u32x4 v[4];
      const int o2 = layer == 0 ? CS_OFF + qo : X2_OFF + bg * 2048 + l * 32;
      const int off[4] = {QP_OFF + layer * 256 * 2048 + qo, QP_OFF + layer * 256 * 2048 + qo + 16, o2, o2 + 16};
      const u32 tb = layer == 0 ? t8 : t16, mb = layer == 0 ? 0xffu : 0xffffu;
      const u32 tg[4] = {t8, t8, tb, tb}, msk[4] = {0xffu, 0xffu, mb, mb};
      gather<4>(rs, off, tg, msk, v);
      STAMP(2 + layer * 8);
      float a[8], c[8];
#pragma unroll
      for (int j = 0; j < 4; ++j) { a[j] = asf(v[0][j]); a[4 + j] = asf(v[1][j]); c[j] = asf(v[2][j]); c[4 + j] = asf(v[3][j]); }
      if (layer == 0) {
        csq = red8(c);
      } else {
        float sm = 0.f;
#pragma unroll
        for (int j = 0; j < 8; ++j) sm += c[j];
        mean = wave_sum(sm) * (1.f / 512.f);
        float sq = 0.f;
#pragma unroll
        for (int j = 0; j < 8; ++j) sq += (c[j] - mean) * (c[j] - mean);
        rstd = __builtin_amdgcn_rsqf(wave_sum(sq) * (1.f / 512.f) + EPS);
        if (w == 0) {
          *(f32x4*)(s_xa + l * 8) = f32x4{c[0], c[1], c[2], c[3]};
          *(f32x4*)(s_xa + l * 8 + 4) = f32x4{c[4], c[5], c[6], c[7]};
        }
      }
      const float qv = (red8(a) - mean * csq) * rstd;
      s_q[w * 64 + (l & 7) * 8 + e8] = (uint16_t)(pk_bf16(qv, qv) & 0xffffu);
      wg_barrier();
      u32x4 qf[4];
#pragma unroll
      for (int kk = 0; kk < 4; ++kk) qf[kk] = *(const u32x4*)(s_q + (i16 >> 2) * 128 + kk * 32 + kg * 8);
      f32x4 s0 = zero4, s1 = zero4;
#pragma unroll
      for (int kk = 0; kk < 4; ++kk) { s0 = mfma(kr[0][kk], qf[kk], s0); s1 = mfma(kr[1][kk], qf[kk], s1); }
      if (layer == 0) tr8<4>(vr, vt);
      const float sc = 0.08838834764831845f * 1.4426950408889634f;
      float m = fmaxf(fmaxf(fmaxf(s0[0], s0[1]), fmaxf(s0[2], s0[3])), fmaxf(fmaxf(s1[0], s1[1]), fmaxf(s1[2], s1[3])));
      m = mx32(mx16(m)) * sc;
      u32x4 pa;
      pa[0] = pk_bf16(__builtin_amdgcn_exp2f(s0[0] * sc - m), __builtin_amdgcn_exp2f(s0[1] * sc - m));
      pa[1] = pk_bf16(__builtin_amdgcn_exp2f(s0[2] * sc - m), __builtin_amdgcn_exp2f(s0[3] * sc - m));
      pa[2] = pk_bf16(__builtin_amdgcn_exp2f(s1[0] * sc - m), __builtin_amdgcn_exp2f(s1[1] * sc - m));
      pa[3] = pk_bf16(__builtin_amdgcn_exp2f(s1[2] * sc - m), __builtin_amdgcn_exp2f(s1[3] * sc - m));
      f32x4 d[8];
#pragma unroll
      for (int c2 = 0; c2 < 8; ++c2) d[c2] = mfma(pa, vt[c2], zero4);
      float ls = 0.f;
#pragma unroll
      for (int j = 0; j < 4; ++j) ls += asf(pa[j] << 16) + asf(pa[j] & 0xffff0000u);
      ls = rs16(ls, ls); ls = rs32(ls, ls);
      if ((l & 0x33) == 0) *(f32x2*)(s_ml + (w * 4 + (l >> 2)) * 2) = f32x2{m, ls};
      *(f32x4*)(s_ow + w * 512 + l * 8) = f32x4{d[0][0], d[1][0], d[2][0], d[3][0]};
      *(f32x4*)(s_ow + w * 512 + l * 8 + 4) = f32x4{d[4][0], d[5][0], d[6][0], d[7][0]};
      wg_barrier();
      const f32x2 ml = *(const f32x2*)(s_ml + ((l >> 3) * 4 + (w >> 1)) * 2);
      const f32x4 oa = *(const f32x4*)(s_ow + (l >> 3) * 512 + w * 64 + (l & 7) * 8);
      const f32x4 ob = *(const f32x4*)(s_ow + (l >> 3) * 512 + w * 64 + (l & 7) * 8 + 4);
      float M = mx32(mx16(fmaxf(ml[0], dpp<ROR8>(ml[0]))));
      M = asf(asu(M) & 0xffffff00u);
      const float cw = __builtin_amdgcn_exp2f(ml[0] - M);
      float L = cw * ml[1];
      L += dpp<ROR8>(L); L = rs16(L, L); L = rs32(L, L);
      const float ov[8] = {oa[0] * cw, oa[1] * cw, oa[2] * cw, oa[3] * cw, ob[0] * cw, ob[1] * cw, ob[2] * cw, ob[3] * cw};
      const float on = red8(ov) * __builtin_amdgcn_rcpf(L);
      __builtin_amdgcn_raw_buffer_store_b32((pk_bf16(on, on) & 0xffff0000u) | t16, rs,
          OP_OFF + ((layer * 32 + bg) * 8 + s) * 2048 + (w * 64 + (l & 7) * 8 + e8) * 4, 0, AUX_SC1);
      if (l == 0 && (w & 1) == 0) {
        u32x2 mlw = {asu(M) | t8, tag8(L, t8)};
        __builtin_amdgcn_raw_buffer_store_b64(mlw, rs, ML_OFF + (layer * 32 + bg) * 256 + ((w >> 1) * 8 + s) * 8, 0, AUX_SC1);
      }
      STAMP(3 + layer * 8);
    }
    {
      if (layer == 0) tr8<4>(wor, wot);
      const int oo = OP_OFF + ((layer * 32 + bg) * 8 + (l & 7)) * 2048 + (w * 64 + ((l >> 3) & 1) * 32 + kg * 8) * 4;
      const int mo = ML_OFF + (layer * 32 + bg) * 256 + ((w >> 1) * 8 + (kg & 1) * 4) * 8;
      const int off[4] = {oo, oo + 16, mo, mo + 16};
      const u32 tg[4] = {t16, t16, t8, t8}, msk[4] = {0xffffu, 0xffffu, 0xffu, 0xffu};
      u32x4 v[4];
      gather<4>(rs, off, tg, msk, v);
      STAMP(4 + layer * 8);
      u32x4 a;
      a[0] = __builtin_amdgcn_perm(v[0][1], v[0][0], 0x07060302u); a[1] = __builtin_amdgcn_perm(v[0][3], v[0][2], 0x07060302u);
      a[2] = __builtin_amdgcn_perm(v[1][1], v[1][0], 0x07060302u); a[3] = __builtin_amdgcn_perm(v[1][3], v[1][2], 0x07060302u);
      f32x4 d[8];
#pragma unroll
      for (int c = 0; c < 8; ++c) d[c] = mfma(a, wot[c], zero4);
      const float m0 = asf(v[2][0]), m1 = asf(v[2][2]), m2 = asf(v[3][0]), m3 = asf(v[3][2]);
      const float M = mx16(fmaxf(fmaxf(m0, m1), fmaxf(m2, m3)));
      float c0 = asf(v[2][1]) * __builtin_amdgcn_exp2f(m0 - M), c1 = asf(v[2][3]) * __builtin_amdgcn_exp2f(m1 - M);
      float c2 = asf(v[3][1]) * __builtin_amdgcn_exp2f(m2 - M), c3 = asf(v[3][3]) * __builtin_amdgcn_exp2f(m3 - M);
      float T = (c0 + c1) + (c2 + c3);
      T = rs16(T, T);
      const float rT = __builtin_amdgcn_rcpf(T);
      c0 *= rT; c1 *= rT; c2 *= rT; c3 *= rT;
      float y[8];
#pragma unroll
      for (int c = 0; c < 8; ++c) y[c] = (d[c][0] * c0 + d[c][1] * c1) + (d[c][2] * c2 + d[c][3] * c3);
      const f32x4 z = {rs16(y[0], y[4]), rs16(y[1], y[5]), rs16(y[2], y[6]), rs16(y[3], y[7])};
      if (((l >> 3) & 1) == (l >> 5)) *(f32x4*)(s_wp + (w * 2 + (l >> 5)) * 64 + (l & 7) * 8 + (kg & 1) * 4) = z;
      wg_barrier();
      if (w == 0) {
        f32x4 acc = *(const f32x4*)(s_wp + (kg * 4) * 64 + i16 * 4);
#pragma unroll
        for (int p = 1; p < 4; ++p) acc += *(const f32x4*)(s_wp + (kg * 4 + p) * 64 + i16 * 4);
        u32x4 o;
#pragma unroll
        for (int j = 0; j < 4; ++j) { float t = rs16(acc[j], acc[j]); o[j] = tag8(rs32(t, t), t8); }
        if (l < 16) stsc1(rs, WP_OFF + (layer * 4 + b) * 16384 + g * 2048 + (s * 64 + l * 4) * 4, o);
      }
      STAMP(5 + layer * 8);
    }
    {
      if (layer == 0) {
        tr8<2>(w1r, w1t); tr8<2>(w2r, w2t);
        f32x4 d[4];
#pragma unroll
        for (int c = 0; c < 4; ++c) d[c] = mfma(ones, w1t[c], zero4);
        if (l < 16) *(f32x4*)(s_hp + (w * 2 + (l >> 3)) * 32 + (l & 7) * 4) = f32x4{d[0][0], d[1][0], d[2][0], d[3][0]};
        wg_barrier();
        float acc = 0.f;
#pragma unroll
        for (int p = 0; p < 8; ++p) acc += s_hp[((l >> 5) * 8 + p) * 32 + (l & 31)];
        cs1 = rs32(acc, acc);
        wg_barrier();
      }
      const int col = w * 64 + ((i16 & 1) * 4 + kg) * 8;
      const f32x4 xp0 = *(const f32x4*)(s_xa + col), xp1 = *(const f32x4*)(s_xa + col + 4);
      const int wo_ = WP_OFF + (layer * 4 + b) * 16384 + (i16 >> 1) * 2048 + col * 4;
      const int off[2] = {wo_, wo_ + 16};
      const u32 tg[2] = {t8, t8}, msk[2] = {0xffu, 0xffu};
      u32x4 v[2];
      gather<2>(rs, off, tg, msk, v);
      STAMP(6 + layer * 8);
      float y[8];
#pragma unroll
      for (int j = 0; j < 8; ++j) {
        float t = asf(v[j >> 2][j & 3]);
        t += dpp<ROR8>(t); t += dpp<ROR4>(t); t += dpp<ROR2>(t);
        y[j] = rbf(t) + (j < 4 ? xp0[j & 3] : xp1[j & 3]);
      }
      u32x4 a;
#pragma unroll
      for (int j = 0; j < 4; ++j) a[j] = pk_bf16(y[2 * j], y[2 * j + 1]);
      f32x4 d[4];
#pragma unroll
      for (int c = 0; c < 4; ++c) d[c] = mfma(a, w1t[c], zero4);
      float sm = 0.f, sq = 0.f;
#pragma unroll
      for (int j = 0; j < 4; ++j) {
        y[2 * j] = asf(a[j] << 16); y[2 * j + 1] = asf(a[j] & 0xffff0000u);
        sm += y[2 * j] + y[2 * j + 1]; sq += y[2 * j] * y[2 * j] + y[2 * j + 1] * y[2 * j + 1];
      }
      sm += dpp<QX1>(sm); sq += dpp<QX1>(sq);
      float st = rs16(sm, sq);
      st = rs32(st, st);
      if (l == 0 || l == 16) s_st[(l >> 4) * 8 + w] = st;
      if (i16 < 2) {
        *(f32x4*)(s_xb + col) = f32x4{y[0], y[1], y[2], y[3]};
        *(f32x4*)(s_xb + col + 4) = f32x4{y[4], y[5], y[6], y[7]};
      }
      if (l < 16) {
        const f32x4 hv = bit3 ? f32x4{d[0][1], d[1][1], d[2][1], d[3][1]} : f32x4{d[0][0], d[1][0], d[2][0], d[3][0]};
        *(f32x4*)(s_hp + (w * 2 + (l >> 3)) * 32 + (l & 7) * 4) = hv;
      }
      wg_barrier();
      if (mlp_on) {
        float acc = 0.f;
#pragma unroll
        for (int p = 0; p < 8; ++p) acc += s_hp[((l >> 5) * 8 + p) * 32 + (l & 31)];
        const f32x4 sa = *(const f32x4*)(s_st), sb = *(const f32x4*)(s_st + 4);
        const f32x4 qa = *(const f32x4*)(s_st + 8), qb = *(const f32x4*)(s_st + 12);
        acc = rs32(acc, acc);
        const float mu = (((sa[0] + sa[1]) + (sa[2] + sa[3])) + ((sb[0] + sb[1]) + (sb[2] + sb[3]))) * (1.f / 512.f);
        const float ex2 = (((qa[0] + qa[1]) + (qa[2] + qa[3])) + ((qb[0] + qb[1]) + (qb[2] + qb[3]))) * (1.f / 512.f);
        const float var = fmaxf(ex2 - mu * mu, 0.f);
        const float r1 = __builtin_amdgcn_rsqf(var + EPS);
        const float r2 = __builtin_amdgcn_rsqf(var * r1 * r1 + EPS);
        const float hp = (acc - mu * cs1) * (r1 * r2) + b1v;
        const float u = hp * (0.7978845608028654f + 0.035677408136300125f * hp * hp);
        const float hv = hp * __builtin_amdgcn_rcpf(1.f + __builtin_amdgcn_exp2f(u * -2.885390081777927f));
        if (l < 32) s_hb[w * 32 + l] = (uint16_t)(pk_bf16(hv, hv) & 0xffffu);
        asm volatile("" ::: "memory");
        const u32x4 ha = *(const u32x4*)(s_hb + w * 32 + kg * 8);
        f32x4 e[4];
#pragma unroll
        for (int c = 0; c < 4; ++c) e[c] = mfma(ha, w2t[c], zero4);
        const u32x4 o = {tag8(e[0][0], t8), tag8(e[1][0], t8), tag8(e[2][0], t8), tag8(e[3][0], t8)};
        if (l < 16) stsc1(rs, MP_OFF + (((layer * 4 + b) * 8 + w) * 56 + g * 7 + s) * 256 + l * 16, o);
      }
      STAMP(7 + layer * 8);
    }
    if (layer == 0) {
      const int xc = s * 64 + (i16 & 1) * 32 + kg * 8;
      const f32x4 xp0 = *(const f32x4*)(s_xb + xc), xp1 = *(const f32x4*)(s_xb + xc + 4);
      const int mo = MP_OFF + ((b * 8 + s) * 56 + w * 7 + kg) * 256 + i16 * 16;
      const int off[2] = {mo, kg < 3 ? mo + 1024 : mo};
      const u32 tg[2] = {t8, t8}, msk[2] = {0xffu, 0xffu};
      u32x4 v[2];
      gather<2>(rs, off, tg, msk, v);
      STAMP(8);
      float y[8];
#pragma unroll
      for (int j = 0; j < 4; ++j) y[j] = asf(v[0][j]) + (kg < 3 ? asf(v[1][j]) : 0.f);
      s_xp[w * 64 + i16 * 4 + (l >> 5) * 2 + (kg & 1)] = rs16(rs32(y[0], y[2]), rs32(y[1], y[3]));
      wg_barrier();
      const f32x4 p0 = *(const f32x4*)(s_xp + (i16 >> 1) * 64 + (i16 & 1) * 32 + kg * 8);
      const f32x4 p1 = *(const f32x4*)(s_xp + (i16 >> 1) * 64 + (i16 & 1) * 32 + kg * 8 + 4);
#pragma unroll
      for (int j = 0; j < 8; ++j) {
        float t = j < 4 ? p0[j & 3] : p1[j & 3];
        t += dpp<ROR8>(t); t += dpp<ROR4>(t); t += dpp<ROR2>(t);
        const float bb = (j & 1) ? asf(b2a[j >> 1] & 0xffff0000u) : asf(b2a[j >> 1] << 16);
        y[j] = rbf(rbf(t) + bb) + (j < 4 ? xp0[j & 3] : xp1[j & 3]);
      }
      u32x4 a;
#pragma unroll
      for (int j = 0; j < 4; ++j) a[j] = pk_bf16(y[2 * j], y[2 * j + 1]);
      if (w == 7 && i16 < 4) {
        const u32 h0 = (i16 & 2) ? a[2] : a[0], h1 = (i16 & 2) ? a[3] : a[1];
        const u32x4 o = {(h0 << 16) | t16, (h0 & 0xffff0000u) | t16, (h1 << 16) | t16, (h1 & 0xffff0000u) | t16};
        stsc1(rs, X2_OFF + bg * 2048 + (xc + (i16 >> 1) * 4) * 4, o);
      }
      s1_core(a, QP_OFF + ((32 + bg) * 8 + s) * 2048);
      STAMP(9);
    } else if (w == 0) {
      const int oc = s * 64 + g * 8 + e8;
      const float xr = s_xb[oc];
      const int mo = MP_OFF + (((4 + b) * 8 + s) * 56 + (l < 56 ? l : 0)) * 256 + g * 32;
      const int off[2] = {mo, mo + 16};
      const u32 tg[2] = {t8, t8}, msk[2] = {0xffu, 0xffu};
      u32x4 v[2];
      gather<2>(rs, off, tg, msk, v);
      STAMP(16);
      float y[8];
#pragma unroll
      for (int j = 0; j < 8; ++j) y[j] = l < 56 ? asf(v[j >> 2][j & 3]) : 0.f;
      float t = red8(y);
      t += dpp<QX1>(t); t += dpp<QX2>(t); t += dpp<0x141>(t);
      const float r = rbf(rbf(t) + b2f) + xr;
      if ((l & 7) == 0) out[b * 512 + oc] = (uint16_t)(pk_bf16(r, r) & 0xffffu);
      if (l == 0 && g == 0 && s == 0) __builtin_amdgcn_raw_buffer_store_b32(tn, rs, EP_OFF + b * 256, 0, AUX_SC1);
      STAMP(17);
    }
  }
}

extern "C" void kernel_launch(void* const* d_in, const int* in_sizes, int n_in, void* d_out, int out_size, void* d_ws,
                              size_t ws_size, hipStream_t stream) {
  (void)in_sizes; (void)n_in; (void)out_size; (void)ws_size;
  fused_decode<<<256, 512, 0, stream>>>((const uint16_t*)d_in[0], (const uint16_t*)d_in[1], (const uint16_t*)d_in[2],
                                        (const uint16_t*)d_in[3], (const uint16_t*)d_in[4], (const uint16_t*)d_in[6],
                                        (const uint16_t*)d_in[7], (const uint16_t*)d_in[8], (const uint16_t*)d_in[9],
                                        (uint16_t*)d_out, (char*)d_ws);
}
```

```cpp
#include <hip/hip_runtime.h>
#include <hip/hip_bf16.h>
#include <stdint.h>

#define DIAG 0

typedef unsigned int u32;
typedef __attribute__((ext_vector_type(4))) unsigned int u32x4;
typedef __attribute__((ext_vector_type(2))) unsigned int u32x2;
typedef __attribute__((ext_vector_type(4))) float f32x4;
typedef __attribute__((ext_vector_type(2))) float f32x2;
typedef __attribute__((ext_vector_type(8))) __bf16 bf16x8;
typedef __attribute__((ext_vector_type(2))) __bf16 bf16x2;

namespace {
constexpr int SK = 8192, WIN = 2048, FF = 1792;
constexpr float EPS = 1e-5f;
constexpr u32 SPIN_CAP = 1u << 17;
constexpr int EP_OFF = 0;
constexpr int QP_OFF = 4096;
constexpr int CS_OFF = QP_OFF + 2 * 256 * 2048;
constexpr int X2_OFF = CS_OFF + 256 * 2048;
constexpr int OP_OFF = X2_OFF + 32 * 2048;
constexpr int ST_OFF = OP_OFF + 2 * 256 * 2048;
constexpr int WP_OFF = ST_OFF + 256 * 128;
constexpr int MP_OFF = WP_OFF + 2 * 4 * 16384;
constexpr int WS_END = MP_OFF + 2 * 4 * 8 * 56 * 256;
constexpr int DG_OFF = WS_END;
constexpr int AUX_SC1 = 16;

__device__ __forceinline__ float asf(u32 x) { return __builtin_bit_cast(float, x); }
__device__ __forceinline__ u32 asu(float x) { return __builtin_bit_cast(u32, x); }
__device__ __forceinline__ u32 pk_bf16(float lo, float hi) {
  f32x2 v = {lo, hi};
  return __builtin_bit_cast(u32, __builtin_convertvector(v, bf16x2));
}
__device__ __forceinline__ float rbf(float x) {
  return asf(pk_bf16(x, x) & 0xffff0000u);
}
__device__ __forceinline__ float fmx(float a, float b) { return __builtin_elementwise_maximum(a, b); }
#define PIN(x) asm volatile("" : "+v"(x))
#define SCHED() __builtin_amdgcn_sched_barrier(0)
template <int CTRL> __device__ __forceinline__ float dpp(float x) {
  return asf(__builtin_amdgcn_update_dpp(0, asu(x), CTRL, 0xf, 0xf, true));
}
constexpr int ROR8 = 0x128, ROR4 = 0x124, ROR2 = 0x122, ROR1 = 0x121, QX1 = 0xB1, QX2 = 0x4E, HMIR = 0x141;
__device__ __forceinline__ float rs32(float a, float b) {
  auto r = __builtin_amdgcn_permlane32_swap(asu(a), asu(b), false, false);
  return asf(r[0]) + asf(r[1]);
}
__device__ __forceinline__ float rs16(float a, float b) {
  auto r = __builtin_amdgcn_permlane16_swap(asu(a), asu(b), false, false);
  return asf(r[0]) + asf(r[1]);
}
__device__ __forceinline__ float rs8(float a, float b, bool bit3) {
  return (bit3 ? b : a) + dpp<ROR8>(bit3 ? a : b);
}
__device__ __forceinline__ float mx32(float a) {
  auto r = __builtin_amdgcn_permlane32_swap(asu(a), asu(a), false, false);
  return fmx(asf(r[0]), asf(r[1]));
}
__device__ __forceinline__ float mx16(float a) {
  auto r = __builtin_amdgcn_permlane16_swap(asu(a), asu(a), false, false);
  return fmx(asf(r[0]), asf(r[1]));
}
__device__ __forceinline__ float wave_sum(float a) {
  a += dpp<ROR8>(a); a += dpp<ROR4>(a); a += dpp<ROR2>(a); a += dpp<ROR1>(a);
  a = rs16(a, a); return rs32(a, a);
}
__device__ __forceinline__ f32x4 mfma(u32x4 a, u32x4 b, f32x4 c) {
  return __builtin_amdgcn_mfma_f32_16x16x32_bf16(__builtin_bit_cast(bf16x8, a), __builtin_bit_cast(bf16x8, b), c, 0, 0, 0);
}
template <int NW, typename V> __device__ __forceinline__ void tr8(const V (&r)[8], u32x4 (&t)[2 * NW]) {
#pragma unroll
  for (int c = 0; c < NW; ++c)
#pragma unroll
    for (int j = 0; j < 4; ++j) {
      t[2 * c][j] = __builtin_amdgcn_perm(r[2 * j + 1][c], r[2 * j][c], 0x05040100u);
      t[2 * c + 1][j] = __builtin_amdgcn_perm(r[2 * j + 1][c], r[2 * j][c], 0x07060302u);
    }
}
__device__ __forceinline__ u32x4 ldsc1(__amdgpu_buffer_rsrc_t rs, int off) {
  return __builtin_amdgcn_raw_buffer_load_b128(rs, off, 0, AUX_SC1);
}
__device__ __forceinline__ void stsc1(__amdgpu_buffer_rsrc_t rs, int off, u32x4 v) {
  __builtin_amdgcn_raw_buffer_store_b128(v, rs, off, 0, AUX_SC1);
}
__device__ __forceinline__ u32 tag8(float v, u32 t8) { return (asu(v) & 0xffffff00u) | t8; }
__device__ __forceinline__ u32 or4(u32x4 v) { return (v[0] | v[1]) | (v[2] | v[3]); }
__device__ __forceinline__ void wg_barrier() {
  asm volatile("s_waitcnt lgkmcnt(0)\n\ts_barrier" ::: "memory");
}
template <int N, bool CLEAN> __device__ __forceinline__ bool untag(u32x4 (&v)[N], u32 tg) {
  u32 bad = 0;
#pragma unroll
  for (int k = 0; k < N; ++k) {
    if (CLEAN) { v[k] ^= tg; bad |= or4(v[k]); }
    else bad |= __builtin_amdgcn_bitop3_b32(v[k][0], v[k][1], tg, 0x7e) | __builtin_amdgcn_bitop3_b32(v[k][2], v[k][3], tg, 0x7e);
  }
  return __all((bad & 0xffu) == 0);
}
template <int N, bool CLEAN = true, typename F>
__device__ __forceinline__ void gather(__amdgpu_buffer_rsrc_t rs, const int (&off)[N], u32 tg, u32x4 (&v)[N], F&& behind) {
#pragma unroll
  for (int k = 0; k < N; ++k) v[k] = ldsc1(rs, off[k]);
  SCHED();
  behind();
  SCHED();
  if (__builtin_expect(!untag<N, CLEAN>(v, tg), 0)) {
    for (u32 spins = 0; spins < SPIN_CAP; ++spins) {
      asm volatile("" ::: "memory");
#pragma unroll
      for (int k = 0; k < N; ++k) v[k] = ldsc1(rs, off[k]);
      if (untag<N, CLEAN>(v, tg)) break;
    }
  }
}
__device__ __forceinline__ f32x2 lo2(f32x4 v) { return __builtin_shufflevector(v, v, 0, 1); }
__device__ __forceinline__ f32x2 hi2(f32x4 v) { return __builtin_shufflevector(v, v, 2, 3); }
__device__ __forceinline__ float sum8(const float (&y)[8]) {
  f32x2 t = (f32x2{y[0], y[1]} + f32x2{y[2], y[3]}) + (f32x2{y[4], y[5]} + f32x2{y[6], y[7]});
  PIN(t);
  return t[0] + t[1];
}
}

#define STAMP(i) do {} while (0)
#define CST(i) do {} while (0)
#define KDELAY {0, 20, 20, 20, 20, 20, 20, 20}
constexpr int kDelay[8] = KDELAY;
#define DLY(i) do { if (kDelay[i] > 0) asm volatile("s_sleep %0" :: "i"(kDelay[i])); } while (0)
#define TUNE_PARAM
#define TUNE_ARG
#define QSHIFT 1
#define LORD 3
#define LBAR 7
#define TEST 0
#define WOPOS 0
#define W1POS 0
#define OSHIFT 1

__global__ void __launch_bounds__(512, 2)
fused_decode(const uint16_t* __restrict__ x, const uint16_t* __restrict__ kc, const uint16_t* __restrict__ vc,
             const uint16_t* __restrict__ wq, const uint16_t* __restrict__ wo, const uint16_t* __restrict__ w1,
             const uint16_t* __restrict__ b1, const uint16_t* __restrict__ w2, const uint16_t* __restrict__ b2,
             uint16_t* __restrict__ out, char* ws TUNE_PARAM) {
  __shared__ __attribute__((aligned(16))) float s_ow[64 * 64];
  __shared__ __attribute__((aligned(16))) float s_ml[4 * 16];
  __shared__ __attribute__((aligned(16))) uint16_t s_q[512];
  __shared__ __attribute__((aligned(16))) uint16_t s_x1[512 + 8];
  __shared__ __attribute__((aligned(16))) float s_hp[2 * 16 * 32];
  __shared__ __attribute__((aligned(16))) float s_st[32];
  __shared__ __attribute__((aligned(16))) uint16_t s_a[8 * 64];
  __shared__ __attribute__((aligned(16))) float s_xp[8 * 64];
  __shared__ __attribute__((aligned(16))) float s_wp[32 * 64];

  const int tid = threadIdx.x, bid = blockIdx.x;
  const int w = __builtin_amdgcn_readfirstlane(tid >> 6);
  const int l = tid & 63, i16 = l & 15, kg = l >> 4;
  const int g = bid & 7, s = (bid >> 3) & 7, b = bid >> 6;
  const int bg = b * 8 + g;
  const bool bit3 = (l & 8) != 0;
  const bool mlp_on = s < 7;
  const int f0 = g * 224 + (mlp_on ? s : 6) * 32;
  const __amdgpu_buffer_rsrc_t rs = __builtin_amdgcn_make_buffer_rsrc((void*)ws, 0, WS_END, 0x00020000);
  STAMP(0);
  const int gq = (g + QSHIFT) & 7, go = (g + OSHIFT) & 7;

  const u32 ep = __builtin_amdgcn_raw_buffer_load_b32(rs, EP_OFF + b * 256, 0, AUX_SC1);
  const u32x4 xfull = *(const u32x4*)(x + b * 512 + l * 8);
  const u32x4 xa0 = *(const u32x4*)(x + b * 512 + s * 64 + (i16 & 1) * 32 + kg * 8);
  const int e8 = (l >> 5) * 4 + ((l >> 4) & 1) * 2 + ((l >> 3) & 1);
  const int c8 = (l & 7) * 8 + e8;
  u32x4 wqr[8], kr[8], vr[8], wor[8];
  u32x2 w1r[8], w2r[8];
  {
    const uint16_t* p = wq + (size_t)(s * 64 + ((l >> 3) & 1) * 32 + kg * 8) * 4096 + gq * 512 + w * 64 + (l & 7) * 8;
#pragma unroll
    for (int r = 0; r < 8; ++r) wqr[r] = *(const u32x4*)(p + r * 4096);
  }
  __builtin_amdgcn_sched_barrier(0);
  asm volatile("s_barrier" ::: "memory");
  __builtin_amdgcn_sched_barrier(0);
  const size_t kvbase = ((size_t)bg * SK + (SK - WIN) + s * 256 + w * 32) * 128;
  {
    const uint16_t* p = kc + kvbase + (i16 >> 2) * 8 * 128 + kg * 32 + (i16 & 3) * 8;
#pragma unroll
    for (int m = 0; m < 8; ++m) kr[m] = __builtin_nontemporal_load((const u32x4*)(p + m * 128));
  }
  auto load_v = [&](int r0, int r1) {
    const uint16_t* p = vc + kvbase + kg * 8 * 128 + i16 * 8;
#pragma unroll
    for (int r = r0; r < r1; ++r) vr[r] = __builtin_nontemporal_load((const u32x4*)(p + r * 128));
  };
  if (LORD == 0) load_v(0, 8);
  __builtin_amdgcn_sched_barrier(0);
  if (LBAR & 1) asm volatile("s_barrier" ::: "memory");
  const float b1v = asf((u32)b1[f0 + (l & 31)] << 16);
  const float b2a = asf((u32)b2[s * 64 + l] << 16);
  const float b2f = asf((u32)b2[s * 64 + g * 8 + e8] << 16);
  u32 xres = (u32)x[b * 512 + w * 64 + c8] << 16;
  __builtin_amdgcn_sched_barrier(0);
  const u32 tn = __builtin_amdgcn_readfirstlane(ep) + 1u;
  const u32 t8 = tn & 0xffu, t16 = tn & 0xffffu;
  u32 t8v = t8;
  PIN(t8v);

  float mean, rstd;
  {
    float xv[8];
#pragma unroll
    for (int j = 0; j < 4; ++j) { xv[2 * j] = asf(xfull[j] << 16); xv[2 * j + 1] = asf(xfull[j] & 0xffff0000u); }
    float sm = 0.f;
#pragma unroll
    for (int j = 0; j < 8; ++j) sm += xv[j];
    mean = wave_sum(sm) * (1.f / 512.f);
    float sq = 0.f;
#pragma unroll
    for (int j = 0; j < 8; ++j) sq += (xv[j] - mean) * (xv[j] - mean);
    rstd = __builtin_amdgcn_rsqf(wave_sum(sq) * (1.f / 512.f) + EPS);
  }
  u32x4 wqt[8];
  tr8<4>(wqr, wqt);
  u32x4 ones = {0x3f803f80u, 0x3f803f80u, 0x3f803f80u, 0x3f803f80u};
  PIN(ones);
  auto none = []() {};
  const f32x4 zero4 = {0.f, 0.f, 0.f, 0.f};

  auto s1_core = [&](u32x4 a, int dst, bool on) {
    f32x4 d[8];
#pragma unroll
    for (int c = 0; c < 8; ++c) d[c] = mfma(a, wqt[c], zero4);
#pragma unroll
    for (int c = 0; c < 8; ++c) PIN(d[c]);
    float keep[4], send[4];
#pragma unroll
    for (int j = 0; j < 4; ++j) send[j] = bit3 ? d[j][1] : d[4 + j][0];
#pragma unroll
    for (int j = 0; j < 4; ++j) keep[j] = bit3 ? d[4 + j][1] : d[j][0];
    SCHED();
    u32x4 o;
#pragma unroll
    for (int j = 0; j < 4; ++j) { float t = keep[j] + dpp<ROR8>(send[j]); PIN(t); o[j] = tag8(t, t8v); }
    if (on) stsc1(rs, dst, o);
  };
  if (tid < 4) ((u32*)(s_x1 + 512))[tid] = 0x3f803f80u;
  s1_core(bit3 ? ones : xa0, (kg == 0 ? QP_OFF : CS_OFF) + ((b * 8 + gq) * 8 + s) * 2048 + w * 256 + i16 * 16, (kg & 1) == 0);
  STAMP(1);
  __builtin_amdgcn_sched_barrier(0);
  if (LBAR & 2) asm volatile("s_barrier" ::: "memory");
  __builtin_amdgcn_sched_barrier(0);
  if (LORD == 2) load_v(0, 4);
  if (LORD == 3) load_v(0, 8);
  __builtin_amdgcn_sched_barrier(0);
  if (LBAR & 4) asm volatile("s_barrier" ::: "memory");
  __builtin_amdgcn_sched_barrier(0);
  auto load_wo = [&]() {
    const uint16_t* p = wo + (size_t)(go * 512 + (((l >> 3) & 1) * 4 + kg) * 64 + w) * 512 + s * 64 + (l & 7) * 8;
#pragma unroll
    for (int r = 0; r < 8; ++r) wor[r] = *(const u32x4*)(p + r * 8 * 512);
  };
  auto load_w1 = [&]() {
    const uint16_t* p = w1 + (size_t)(w * 64 + ((l >> 3) & 1) * 32 + kg * 8) * FF + f0 + (l & 7) * 4;
#pragma unroll
    for (int r = 0; r < 8; ++r) w1r[r] = *(const u32x2*)(p + r * FF);
  };
  auto load_w2 = [&]() {
    const uint16_t* p2 = w2 + (size_t)(f0 + kg * 8) * 512 + w * 64 + i16 * 4;
#pragma unroll
    for (int r = 0; r < 8; ++r) w2r[r] = *(const u32x2*)(p2 + r * 512);
  };

  const int xo = X2_OFF + bg * 2048 + w * 256 + c8 * 4;
  u32x4 vt[8], wot[8], w1t[4], w2t[4];
  float csq = 0.f, cs1 = 0.f;
  auto red8 = [&](const float (&v)[8]) {
    const float u0 = rs32(v[0], v[4]), u1 = rs32(v[1], v[5]), u2 = rs32(v[2], v[6]), u3 = rs32(v[3], v[7]);
    return rs8(rs16(u0, u2), rs16(u1, u3), bit3);
  };

#pragma unroll
  for (int layer = 0; layer < 2; ++layer) {
    {
      const int qo = (bg * 8 + (l >> 3)) * 2048 + w * 256 + (l & 7) * 16;
      int od = OP_OFF + ((layer * 32 + bg) * 8 + s) * 2048 + w * 256 + ((l >> 2) & 1) * 128 + (l >> 3) * 16 + (l & 3) * 4;
      PIN(od);
      float a[8];
      DLY(layer * 4);
      if (layer == 0) {
        u32x4 v[4];
        const int off[4] = {QP_OFF + qo, QP_OFF + qo + 128, CS_OFF + qo, CS_OFF + qo + 128};
        gather<4>(rs, off, t8, v, [&]() { if (WOPOS == 0) load_wo(); });
        STAMP(2);
        float c[8];
#pragma unroll
        for (int j = 0; j < 4; ++j) { a[j] = asf(v[0][j]); a[4 + j] = asf(v[1][j]); c[j] = asf(v[2][j]); c[4 + j] = asf(v[3][j]); }
        csq = red8(c);
      } else {
        u32x4 v[3];
        const int off[3] = {ST_OFF + (bg * 8 + (l >> 3)) * 128 + (l & 7) * 16, QP_OFF + 256 * 2048 + qo, QP_OFF + 256 * 2048 + qo + 128};
        gather<3>(rs, off, t8, v, [&]() { xres = __builtin_amdgcn_raw_buffer_load_b32(rs, xo, 0, AUX_SC1); });
        STAMP(10);
        CST(0);
#pragma unroll
        for (int j = 0; j < 4; ++j) { a[j] = asf(v[1][j]); a[4 + j] = asf(v[2][j]); }
        float u = rs32(asf(v[0][0]) + asf(v[0][1]), asf(v[0][2]) + asf(v[0][3]));
        u = rs16(u, u);
        u += dpp<ROR8>(u);
        auto r = __builtin_amdgcn_permlane32_swap(asu(u), asu(u), false, false);
        mean = asf(r[0]) * (1.f / 512.f);
        rstd = __builtin_amdgcn_rsqf(fmx(asf(r[1]) * (1.f / 512.f) - mean * mean, 0.f) + EPS);
      }
      const float qv = (red8(a) - mean * csq) * rstd;
      s_q[w * 64 + c8] = (uint16_t)(pk_bf16(qv, qv) & 0xffffu);
      CST(1);
      wg_barrier();
      CST(2);
      const u32x4 qf = *(const u32x4*)(s_q + (i16 >> 2) * 128 + kg * 32 + (i16 & 3) * 8);
      f32x4 d[8];
#pragma unroll
      for (int m = 0; m < 8; ++m) d[m] = mfma(kr[m], qf, zero4);
      SCHED();
      float sv[8];
#pragma unroll
      for (int m = 0; m < 8; ++m) { sv[m] = d[m][0] + dpp<0x55>(d[m][1]); PIN(sv[m]); }
#pragma unroll
      for (int m = 0; m < 8; ++m) { sv[m] += dpp<0xAA>(d[m][2]); PIN(sv[m]); }
#pragma unroll
      for (int m = 0; m < 8; ++m) { sv[m] += dpp<0xFF>(d[m][3]); PIN(sv[m]); }
      if (layer == 0) tr8<4>(vr, vt);
      CST(3);
      const float sc = 0.08838834764831845f * 1.4426950408889634f;
      float m = fmx(fmx(fmx(fmx(sv[0], sv[1]), sv[2]), fmx(fmx(sv[3], sv[4]), sv[5])), fmx(sv[6], sv[7]));
      m = mx32(mx16(m)) * sc;
      u32x4 pa;
#pragma unroll
      for (int j = 0; j < 4; ++j) {
        const f32x2 e = f32x2{sv[2 * j], sv[2 * j + 1]} * sc - m;
        pa[j] = pk_bf16(__builtin_amdgcn_exp2f(e[0]), __builtin_amdgcn_exp2f(e[1]));
      }
      CST(4);
      f32x4 dl = mfma(pa, ones, zero4);
      SCHED();
#pragma unroll
      for (int c2 = 0; c2 < 8; ++c2) d[c2] = mfma(pa, vt[c2], zero4);
      PIN(dl);
#pragma unroll
      for (int c2 = 0; c2 < 8; ++c2) PIN(d[c2]);
      if (i16 == 4 * kg) { s_ml[kg * 16 + w] = m; s_ml[kg * 16 + 8 + w] = dl[0]; }
#pragma unroll
      for (int c2 = 0; c2 < 8; ++c2) s_ow[(w * 8 + c2) * 64 + l] = d[c2][0];
      CST(5);
      wg_barrier();
      CST(6);
      const f32x4 m0 = *(const f32x4*)(s_ml + kg * 16), m1 = *(const f32x4*)(s_ml + kg * 16 + 4);
      const f32x4 l0 = *(const f32x4*)(s_ml + kg * 16 + 8), l1 = *(const f32x4*)(s_ml + kg * 16 + 12);
      float ov[8];
#pragma unroll
      for (int u = 0; u < 8; ++u) ov[u] = s_ow[(u * 8 + w) * 64 + l];
      const float M = fmx(fmx(fmx(fmx(m0[0], m0[1]), m0[2]), fmx(fmx(m0[3], m1[0]), m1[1])), fmx(m1[2], m1[3]));
      const f32x2 mp[4] = {lo2(m0), hi2(m0), lo2(m1), hi2(m1)}, lp[4] = {lo2(l0), hi2(l0), lo2(l1), hi2(l1)};
      f32x2 L2, o2;
#pragma unroll
      for (int u = 0; u < 4; ++u) {
        const f32x2 e = mp[u] - M;
        const f32x2 c = {__builtin_amdgcn_exp2f(e[0]), __builtin_amdgcn_exp2f(e[1])};
        const f32x2 ou = {ov[2 * u], ov[2 * u + 1]};
        L2 = u ? c * lp[u] + L2 : c * lp[u];
        o2 = u ? c * ou + o2 : c * ou;
      }
      PIN(L2); PIN(o2);
      const float L = L2[0] + L2[1];
      const float on = (o2[0] + o2[1]) * __builtin_amdgcn_rcpf(L);
      const float lw = M + __builtin_amdgcn_logf(L);
      __builtin_amdgcn_raw_buffer_store_b32(__builtin_amdgcn_perm(pk_bf16(on, on), asu(lw), 0x0706000cu | ((l & 3) << 8)) | t8, rs, od, 0, AUX_SC1);
      STAMP(3 + layer * 8);
      CST(7);
    }
    {
      if (layer == 0 && WOPOS == 1) { SCHED(); load_wo(); SCHED(); }
      const int oo = OP_OFF + ((layer * 32 + b * 8 + go) * 8 + (l & 7)) * 2048 + w * 256 + (((l >> 3) & 1) * 4 + kg) * 16;
      const int off[2] = {oo, oo + 128};
      int wd = WP_OFF + (layer * 4 + b) * 16384 + g * 2048 + s * 256 + (l & 1) * 128 + (i16 >> 1) * 16 + (l >> 5) * 8 + (kg & 1) * 4;
      PIN(wd);
      u32x4 v[2];
      DLY(1 + layer * 4);
      if (layer == 0) gather<2, false>(rs, off, t8, v, [&]() {
        if (W1POS == 0) { load_w1(); SCHED(); }
        tr8<4>(wor, wot);
#pragma unroll
        for (int c = 0; c < 8; ++c) PIN(wot[c]);
      }); else gather<2, false>(rs, off, t8, v, [&]() {
        if (__builtin_expect(!__all(((xres ^ t16) & 0xffffu) == 0), 0)) {
          for (u32 spins = 0; spins < SPIN_CAP; ++spins) {
            asm volatile("" ::: "memory");
            xres = __builtin_amdgcn_raw_buffer_load_b32(rs, xo, 0, AUX_SC1);
            if (__all(((xres ^ t16) & 0xffffu) == 0)) break;
          }
        }
        xres &= 0xffff0000u;
        PIN(xres);
      });
      STAMP(4 + layer * 8);
      CST(8);
      const float lw = asf(__builtin_amdgcn_perm(__builtin_amdgcn_perm(v[0][3], v[0][2], 0x05010000u), __builtin_amdgcn_perm(v[0][1], v[0][0], 0x00000501u), 0x07060100u));
      float M = fmx(lw, dpp<QX1>(lw)); M = fmx(M, dpp<QX2>(M)); M = fmx(M, dpp<HMIR>(M));
      float cw = __builtin_amdgcn_exp2f(lw - M);
      float T = cw + dpp<QX1>(cw); T += dpp<QX2>(T); T += dpp<HMIR>(T);
      cw *= __builtin_amdgcn_rcpf(T);
      u32x4 a;
#pragma unroll
      for (int j = 0; j < 4; ++j)
        a[j] = pk_bf16(asf(v[j >> 1][(j & 1) * 2] & 0xffff0000u) * cw, asf(v[j >> 1][(j & 1) * 2 + 1] & 0xffff0000u) * cw);
      f32x4 d[8];
#pragma unroll
      for (int c = 0; c < 8; ++c) d[c] = mfma(a, wot[c], zero4);
      f32x4 z[2];
#pragma unroll
      for (int c = 0; c < 8; ++c) { f32x2 t = lo2(d[c]) + hi2(d[c]); PIN(t); float r = t[0] + t[1]; PIN(r); z[c >> 2][c & 3] = r; }
      if (((l >> 3) & 1) == (l >> 5)) {
        *(f32x4*)(s_wp + (w * 4 + kg) * 64 + (l & 7) * 8) = z[0];
        *(f32x4*)(s_wp + (w * 4 + kg) * 64 + (l & 7) * 8 + 4) = z[1];
      }
      CST(9);
      wg_barrier();
      CST(10);
      if (w == 0) {
        f32x4 p[8];
#pragma unroll
        for (int k = 0; k < 8; ++k) p[k] = *(const f32x4*)(s_wp + (kg * 8 + k) * 64 + i16 * 4);
        const f32x4 acc = ((p[0] + p[1]) + (p[2] + p[3])) + ((p[4] + p[5]) + (p[6] + p[7]));
        const float t = rs16(rs32(acc[0], acc[2]), rs32(acc[1], acc[3]));
        __builtin_amdgcn_raw_buffer_store_b32(tag8(t, t8v), rs, wd, 0, AUX_SC1);
      }
      STAMP(5 + layer * 8);
      CST(11);
    }
    {
      if (layer == 0 && W1POS == 1) { SCHED(); load_w1(); SCHED(); }
      const int wo_ = WP_OFF + (layer * 4 + b) * 16384 + (l >> 3) * 2048 + w * 256 + (l & 7) * 16;
      const int off[2] = {wo_, wo_ + 128};
      int md = MP_OFF + (((layer * 4 + b) * 8 + w) * 56 + g * 7 + s) * 256 + l * 16;
      PIN(md);
      u32x4 v[2];
      DLY(2 + layer * 4);
      if (layer == 0) gather<2>(rs, off, t8, v, [&]() {
        load_w2();
        SCHED();
        tr8<2>(w1r, w1t);
#pragma unroll
        for (int c = 0; c < 4; ++c) PIN(w1t[c]);
      }); else gather<2>(rs, off, t8, v, none);
      STAMP(6 + layer * 8);
      CST(12);
      float y[8];
#pragma unroll
      for (int j = 0; j < 8; ++j) y[j] = asf(v[j >> 2][j & 3]);
      const float x1 = rbf(red8(y)) + asf(xres);
      s_x1[w * 64 + c8] = (uint16_t)(pk_bf16(x1, x1) & 0xffffu);
      asm volatile("" ::: "memory");
      const int xf = w * 64 + (i16 & 1) * 32 + kg * 8;
      const u32x4 am = *(const u32x4*)(s_x1 + (bit3 ? 512 : xf));
      const u32x4 bx = *(const u32x4*)(s_x1 + xf);
      f32x4 d[4];
#pragma unroll
      for (int c = 0; c < 4; ++c) d[c] = mfma(am, w1t[c], zero4);
      f32x4 gr = mfma(am, bx, zero4);
#pragma unroll
      for (int c = 0; c < 4; ++c) PIN(d[c]);
      if (l < 16 || (layer == 0 && kg == 2)) {
        const f32x4 hv = bit3 ? f32x4{d[0][1], d[1][1], d[2][1], d[3][1]} : f32x4{d[0][0], d[1][0], d[2][0], d[3][0]};
        *(f32x4*)(s_hp + (l >> 5) * 512 + (w * 2 + ((l >> 3) & 1)) * 32 + (l & 7) * 4) = hv;
      }
      if ((l & 30) == 0) s_st[(l >> 5) * 16 + w * 2 + (l & 1)] = (l & 1) ? gr[1] : gr[0];
      if (layer == 0) tr8<2>(w2r, w2t);
      wg_barrier();
      if (mlp_on) {
        float st = s_st[l & 31];
        float hp8[8];
#pragma unroll
        for (int p = 0; p < 8; ++p) hp8[p] = s_hp[((l >> 5) * 8 + p) * 32 + (l & 31)];
        if (layer == 0) {
          float ac = 0.f;
#pragma unroll
          for (int p = 0; p < 8; ++p) ac += s_hp[512 + ((l >> 5) * 8 + p) * 32 + (l & 31)];
          cs1 = rs32(ac, ac);
        }
        st += dpp<ROR8>(st); st += dpp<ROR4>(st); st += dpp<ROR2>(st); st += dpp<ROR1>(st);
        float acc = sum8(hp8);
        acc = rs32(acc, acc);
        auto r = __builtin_amdgcn_permlane16_swap(asu(st), asu(st), false, false);
        const float mu = asf(r[1]) * (1.f / 512.f);
        const float var = fmx(asf(r[0]) * (1.f / 512.f) - mu * mu, 0.f);
        const float r12 = __builtin_amdgcn_rsqf(var + EPS * (var + EPS));
        const float hp = (acc - mu * cs1) * r12 + b1v;
        const float u = hp * (-2.302208198144325f - 0.1029432395800235f * (hp * hp));
        const float hv = hp * __builtin_amdgcn_rcpf(1.f + __builtin_amdgcn_exp2f(u));
        s_a[w * 64 + l] = (uint16_t)(pk_bf16(hv, hv) & 0xffffu);
        asm volatile("" ::: "memory");
        const u32x4 ha = *(const u32x4*)(s_a + w * 64 + kg * 8);
        f32x4 e[4];
#pragma unroll
        for (int c = 0; c < 4; ++c) e[c] = mfma(ha, w2t[c], zero4);
#pragma unroll
        for (int c = 0; c < 4; ++c) PIN(e[c]);
        const u32x4 o = {tag8(e[0][0], t8v), tag8(e[1][0], t8v), tag8(e[2][0], t8v), tag8(e[3][0], t8v)};
        if (l < 16) stsc1(rs, md, o);
      }
      STAMP(7 + layer * 8);
      CST(13);
    }
    if (layer == 0) {
      float xr = asf((u32)s_x1[s * 64 + l] << 16);
      PIN(xr);
      const int mo = MP_OFF + ((b * 8 + s) * 56 + w * 7 + kg) * 256 + i16 * 16;
      const int off[2] = {mo, kg < 3 ? mo + 1024 : mo};
      float two = kg < 3 ? 1.f : 0.f;
      int qd = QP_OFF + ((32 + b * 8 + gq) * 8 + s) * 2048 + w * 256 + i16 * 16;
      int sd = ST_OFF + ((b * 8 + gq) * 8 + s) * 128 + (l & 7) * 16 + (l >> 5) * 8;
      int xpi = w * 64 + i16 * 4 + (l >> 5) * 2 + (kg & 1), sai = w * 64 + (i16 & 1) * 32 + kg * 8;
      float b2p = b2a;
      PIN(two); PIN(qd); PIN(sd); PIN(xpi); PIN(sai); PIN(b2p);
      u32x4 v[2];
      DLY(3);
      gather<2>(rs, off, t8, v, none);
      STAMP(8);
      float y[8];
#pragma unroll
      for (int j = 0; j < 4; ++j) y[j] = asf(v[0][j]) + two * asf(v[1][j]);
      s_xp[xpi] = rs16(rs32(y[0], y[2]), rs32(y[1], y[3]));
      wg_barrier();
#pragma unroll
      for (int u = 0; u < 8; ++u) y[u] = s_xp[u * 64 + l];
      const float x2f = rbf(rbf(sum8(y)) + b2p) + xr;
      const u32 x2 = pk_bf16(x2f, x2f);
      s_a[w * 64 + l] = (uint16_t)(x2 & 0xffffu);
      asm volatile("" ::: "memory");
      const u32x4 a = *(const u32x4*)(s_a + sai);
      if (w == 6) {
        const float xv = asf(x2 & 0xffff0000u);
        float u = rs32(xv, xv * xv);
        u = rs16(u, u);
        u += dpp<ROR8>(u); u += dpp<ROR4>(u); u += dpp<ROR2>(u); u += dpp<ROR1>(u);
        const float hi = asf(asu(u) & 0xffffff00u);
        const u32x2 o = {asu(hi) | t8, tag8(u - hi, t8v)};
        if ((l & 24) == 0) __builtin_amdgcn_raw_buffer_store_b64(o, rs, sd, 0, AUX_SC1);
      }
      s1_core(a, qd, l < 16);
      if (w == 7) __builtin_amdgcn_raw_buffer_store_b32((x2 & 0xffff0000u) | t16, rs, X2_OFF + (b * 8 + gq) * 2048 + s * 256 + l * 4, 0, AUX_SC1);
      STAMP(9);
    } else if (w == 0) {
      const int oc = s * 64 + g * 8 + e8;
      const float xr = asf((u32)s_x1[oc] << 16);
      const int mo = MP_OFF + (((4 + b) * 8 + s) * 56 + (l < 56 ? l : 0)) * 256 + g * 32;
      const int off[2] = {mo, mo + 16};
      u32x4 v[2];
      DLY(7);
      gather<2>(rs, off, t8, v, none);
      STAMP(16);
      float y[8];
#pragma unroll
      for (int j = 0; j < 8; ++j) y[j] = l < 56 ? asf(v[j >> 2][j & 3]) : 0.f;
      float t = red8(y);
      t += dpp<QX1>(t); t += dpp<QX2>(t); t += dpp<HMIR>(t);
      const float r = rbf(rbf(t) + b2f) + xr;
      if ((l & 7) == 0) out[b * 512 + oc] = (uint16_t)(pk_bf16(r, r) & 0xffffu);
      if (l == 0 && g == 0 && s == 0) __builtin_amdgcn_raw_buffer_store_b32(tn, rs, EP_OFF + b * 256, 0, AUX_SC1);
      STAMP(17);
    }
  }
}

extern "C" void kernel_launch(void* const* d_in, const int* in_sizes, int n_in, void* d_out, int out_size, void* d_ws,
                              size_t ws_size, hipStream_t stream) {
  (void)in_sizes; (void)n_in; (void)out_size; (void)ws_size;
  fused_decode<<<256, 512, 0, stream>>>((const uint16_t*)d_in[0], (const uint16_t*)d_in[1], (const uint16_t*)d_in[2],
                                        (const uint16_t*)d_in[3], (const uint16_t*)d_in[4], (const uint16_t*)d_in[6],
                                        (const uint16_t*)d_in[7], (const uint16_t*)d_in[8], (const uint16_t*)d_in[9],
                                        (uint16_t*)d_out, (char*)d_ws TUNE_ARG);
}
```

```cpp
#include <hip/hip_runtime.h>
#include <hip/hip_bf16.h>
#include <stdint.h>

#define DIAG 0

typedef unsigned int u32;
typedef __attribute__((ext_vector_type(4))) unsigned int u32x4;
typedef __attribute__((ext_vector_type(2))) unsigned int u32x2;
typedef __attribute__((ext_vector_type(4))) float f32x4;
typedef __attribute__((ext_vector_type(2))) float f32x2;
typedef __attribute__((ext_vector_type(8))) __bf16 bf16x8;
typedef __attribute__((ext_vector_type(2))) __bf16 bf16x2;

namespace {
constexpr int SK = 8192, WIN = 2048, FF = 1792;
constexpr float EPS = 1e-5f;
constexpr u32 SPIN_CAP = 1u << 17;
constexpr int EP_OFF = 0;
constexpr int QP_OFF = 4096;
constexpr int CS_OFF = QP_OFF + 2 * 256 * 2048;
constexpr int X2_OFF = CS_OFF + 256 * 2048;
constexpr int OP_OFF = X2_OFF + 32 * 2048;
constexpr int ST_OFF = OP_OFF + 2 * 256 * 2048;
constexpr int WP_OFF = ST_OFF + 256 * 128;
constexpr int MP_OFF = WP_OFF + 2 * 4 * 16384;
constexpr int WS_END = MP_OFF + 2 * 4 * 8 * 56 * 256;
constexpr int DG_OFF = WS_END;
constexpr int AUX_SC1 = 16;

__device__ __forceinline__ float asf(u32 x) { return __builtin_bit_cast(float, x); }
__device__ __forceinline__ u32 asu(float x) { return __builtin_bit_cast(u32, x); }
__device__ __forceinline__ u32 pk_bf16(float lo, float hi) {
  f32x2 v = {lo, hi};
  return __builtin_bit_cast(u32, __builtin_convertvector(v, bf16x2));
}
__device__ __forceinline__ float rbf(float x) {
  return asf(pk_bf16(x, x) & 0xffff0000u);
}
__device__ __forceinline__ float fmx(float a, float b) { return __builtin_elementwise_maximum(a, b); }
#define PIN(x) asm volatile("" : "+v"(x))
#define SCHED() __builtin_amdgcn_sched_barrier(0)
template <int CTRL> __device__ __forceinline__ float dpp(float x) {
  return asf(__builtin_amdgcn_update_dpp(0, asu(x), CTRL, 0xf, 0xf, true));
}
constexpr int ROR8 = 0x128, ROR4 = 0x124, ROR2 = 0x122, ROR1 = 0x121, QX1 = 0xB1, QX2 = 0x4E, HMIR = 0x141;
__device__ __forceinline__ float rs32(float a, float b) {
  auto r = __builtin_amdgcn_permlane32_swap(asu(a), asu(b), false, false);
  return asf(r[0]) + asf(r[1]);
}
__device__ __forceinline__ float rs16(float a, float b) {
  auto r = __builtin_amdgcn_permlane16_swap(asu(a), asu(b), false, false);
  return asf(r[0]) + asf(r[1]);
}
__device__ __forceinline__ float rs8(float a, float b, bool bit3) {
  return (bit3 ? b : a) + dpp<ROR8>(bit3 ? a : b);
}
__device__ __forceinline__ float mx32(float a) {
  auto r = __builtin_amdgcn_permlane32_swap(asu(a), asu(a), false, false);
  return fmx(asf(r[0]), asf(r[1]));
}
__device__ __forceinline__ float mx16(float a) {
  auto r = __builtin_amdgcn_permlane16_swap(asu(a), asu(a), false, false);
  return fmx(asf(r[0]), asf(r[1]));
}
__device__ __forceinline__ float wave_sum(float a) {
  a += dpp<ROR8>(a); a += dpp<ROR4>(a); a += dpp<ROR2>(a); a += dpp<ROR1>(a);
  a = rs16(a, a); return rs32(a, a);
}
__device__ __forceinline__ f32x4 mfma(u32x4 a, u32x4 b, f32x4 c) {
  return __builtin_amdgcn_mfma_f32_16x16x32_bf16(__builtin_bit_cast(bf16x8, a), __builtin_bit_cast(bf16x8, b), c, 0, 0, 0);
}
template <int NW, typename V> __device__ __forceinline__ void tr8(const V (&r)[8], u32x4 (&t)[2 * NW]) {
#pragma unroll
  for (int c = 0; c < NW; ++c)
#pragma unroll
    for (int j = 0; j < 4; ++j) {
      t[2 * c][j] = __builtin_amdgcn_perm(r[2 * j + 1][c], r[2 * j][c], 0x05040100u);
      t[2 * c + 1][j] = __builtin_amdgcn_perm(r[2 * j + 1][c], r[2 * j][c], 0x07060302u);
    }
}
__device__ __forceinline__ u32x4 ldsc1(__amdgpu_buffer_rsrc_t rs, int off) {
  return __builtin_amdgcn_raw_buffer_load_b128(rs, off, 0, AUX_SC1);
}
__device__ __forceinline__ void stsc1(__amdgpu_buffer_rsrc_t rs, int off, u32x4 v) {
  __builtin_amdgcn_raw_buffer_store_b128(v, rs, off, 0, AUX_SC1);
}
__device__ __forceinline__ u32 tag8(float v, u32 t8) { return (asu(v) & 0xffffff00u) | t8; }
__device__ __forceinline__ u32 or4(u32x4 v) { return (v[0] | v[1]) | (v[2] | v[3]); }
__device__ __forceinline__ void wg_barrier() {
  asm volatile("s_waitcnt lgkmcnt(0)\n\ts_barrier" ::: "memory");
}
template <int N, bool CLEAN> __device__ __forceinline__ bool untag(u32x4 (&v)[N], u32 tg) {
  u32 bad = 0;
#pragma unroll
  for (int k = 0; k < N; ++k) {
    if (CLEAN) { v[k] ^= tg; bad |= or4(v[k]); }
    else bad |= __builtin_amdgcn_bitop3_b32(v[k][0], v[k][1], tg, 0x7e) | __builtin_amdgcn_bitop3_b32(v[k][2], v[k][3], tg, 0x7e);
  }
  return __all((bad & 0xffu) == 0);
}
template <int N, bool CLEAN = true, typename F>
__device__ __forceinline__ void gather(__amdgpu_buffer_rsrc_t rs, const int (&off)[N], u32 tg, u32x4 (&v)[N], F&& behind) {
#pragma unroll
  for (int k = 0; k < N; ++k) v[k] = ldsc1(rs, off[k]);
  SCHED();
  behind();
  SCHED();
  if (__builtin_expect(!untag<N, CLEAN>(v, tg), 0)) {
    for (u32 spins = 0; spins < SPIN_CAP; ++spins) {
      asm volatile("" ::: "memory");
#pragma unroll
      for (int k = 0; k < N; ++k) v[k] = ldsc1(rs, off[k]);
      if (untag<N, CLEAN>(v, tg)) break;
    }
  }
}
__device__ __forceinline__ f32x2 lo2(f32x4 v) { return __builtin_shufflevector(v, v, 0, 1); }
__device__ __forceinline__ f32x2 hi2(f32x4 v) { return __builtin_shufflevector(v, v, 2, 3); }
__device__ __forceinline__ float sum8(const float (&y)[8]) {
  f32x2 t = (f32x2{y[0], y[1]} + f32x2{y[2], y[3]}) + (f32x2{y[4], y[5]} + f32x2{y[6], y[7]});
  PIN(t);
  return t[0] + t[1];
}
}

#define STAMP(i) do {} while (0)
#define CST(i) do {} while (0)
#define KDELAY {0, 18, 18, 18, 18, 18, 18, 18}
constexpr int kDelay[8] = KDELAY;
#define DLY(i) do { if (kDelay[i] > 0) asm volatile("s_sleep %0" :: "i"(kDelay[i])); } while (0)
#define TUNE_PARAM
#define TUNE_ARG
#define QSHIFT 1
#define LORD 3
#define LBAR 7
#define TEST 0
#define WOPOS 0
#define W1POS 1
#define OSHIFT 1

__global__ void __launch_bounds__(512, 2)
fused_decode(const uint16_t* __restrict__ x, const uint16_t* __restrict__ kc, const uint16_t* __restrict__ vc,
             const uint16_t* __restrict__ wq, const uint16_t* __restrict__ wo, const uint16_t* __restrict__ w1,
             const uint16_t* __restrict__ b1, const uint16_t* __restrict__ w2, const uint16_t* __restrict__ b2,
             uint16_t* __restrict__ out, char* ws TUNE_PARAM) {
  __shared__ __attribute__((aligned(16))) float s_ow[64 * 64];
  __shared__ __attribute__((aligned(16))) float s_ml[4 * 16];
  __shared__ __attribute__((aligned(16))) uint16_t s_q[512];
  __shared__ __attribute__((aligned(16))) uint16_t s_x1[512 + 8];
  __shared__ __attribute__((aligned(16))) float s_hp[2 * 16 * 32];
  __shared__ __attribute__((aligned(16))) float s_st[32];
  __shared__ __attribute__((aligned(16))) uint16_t s_a[8 * 64];
  __shared__ __attribute__((aligned(16))) float s_xp[8 * 64];
  __shared__ __attribute__((aligned(16))) float s_wp[32 * 64];

  const int tid = threadIdx.x, bid = blockIdx.x;
  const int w = __builtin_amdgcn_readfirstlane(tid >> 6);
  const int l = tid & 63, i16 = l & 15, kg = l >> 4;
  const int g = bid & 7, s = (bid >> 3) & 7, b = bid >> 6;
  const int bg = b * 8 + g;
  const bool bit3 = (l & 8) != 0;
  const bool mlp_on = s < 7;
  const int f0 = g * 224 + (mlp_on ? s : 6) * 32;
  const __amdgpu_buffer_rsrc_t rs = __builtin_amdgcn_make_buffer_rsrc((void*)ws, 0, WS_END, 0x00020000);
  STAMP(0);
  const int gq = (g + QSHIFT) & 7, go = (g + OSHIFT) & 7;

  const u32 ep = __builtin_amdgcn_raw_buffer_load_b32(rs, EP_OFF + b * 256, 0, AUX_SC1);
  const u32x4 xfull = *(const u32x4*)(x + b * 512 + l * 8);
  const u32x4 xa0 = *(const u32x4*)(x + b * 512 + s * 64 + (i16 & 1) * 32 + kg * 8);
  const int e8 = (l >> 5) * 4 + ((l >> 4) & 1) * 2 + ((l >> 3) & 1);
  const int c8 = (l & 7) * 8 + e8;
  u32x4 wqr[8], kr[8], vr[8], wor[8];
  u32x2 w1r[8], w2r[8];
  {
    const uint16_t* p = wq + (size_t)(s * 64 + ((l >> 3) & 1) * 32 + kg * 8) * 4096 + gq * 512 + w * 64 + (l & 7) * 8;
#pragma unroll
    for (int r = 0; r < 8; ++r) wqr[r] = *(const u32x4*)(p + r * 4096);
  }
  __builtin_amdgcn_sched_barrier(0);
  asm volatile("s_barrier" ::: "memory");
  __builtin_amdgcn_sched_barrier(0);
  const size_t kvbase = ((size_t)bg * SK + (SK - WIN) + s * 256 + w * 32) * 128;
  {
    const uint16_t* p = kc + kvbase + (i16 >> 2) * 8 * 128 + kg * 32 + (i16 & 3) * 8;
#pragma unroll
    for (int m = 0; m < 8; ++m) kr[m] = *((const u32x4*)(p + m * 128));
  }
  auto load_v = [&](int r0, int r1) {
    const uint16_t* p = vc + kvbase + kg * 8 * 128 + i16 * 8;
#pragma unroll
    for (int r = r0; r < r1; ++r) vr[r] = *((const u32x4*)(p + r * 128));
  };
  if (LORD == 0) load_v(0, 8);
  __builtin_amdgcn_sched_barrier(0);
  if (LBAR & 1) asm volatile("s_barrier" ::: "memory");
  const float b1v = asf((u32)b1[f0 + (l & 31)] << 16);
  const float b2a = asf((u32)b2[s * 64 + l] << 16);
  const float b2f = asf((u32)b2[s * 64 + g * 8 + e8] << 16);
  u32 xres = (u32)x[b * 512 + w * 64 + c8] << 16;
  __builtin_amdgcn_sched_barrier(0);
  const u32 tn = __builtin_amdgcn_readfirstlane(ep) + 1u;
  const u32 t8 = tn & 0xffu, t16 = tn & 0xffffu;
  u32 t8v = t8;
  PIN(t8v);

  u32x4 ya0;
  {
    float xv[8];
#pragma unroll
    for (int j = 0; j < 4; ++j) { xv[2 * j] = asf(xfull[j] << 16); xv[2 * j + 1] = asf(xfull[j] & 0xffff0000u); }
    float sm = 0.f;
#pragma unroll
    for (int j = 0; j < 8; ++j) sm += xv[j];
    const float mean = wave_sum(sm) * (1.f / 512.f);
    float sq = 0.f;
#pragma unroll
    for (int j = 0; j < 8; ++j) sq += (xv[j] - mean) * (xv[j] - mean);
    const float mb = rbf(mean);
    const float rsd = __builtin_amdgcn_rcpf(rbf(__builtin_amdgcn_sqrtf(rbf(rbf(wave_sum(sq) * (1.f / 512.f)) + EPS))));
#pragma unroll
    for (int j = 0; j < 4; ++j) {
      const u32 c = pk_bf16(asf(xa0[j] << 16) - mb, asf(xa0[j] & 0xffff0000u) - mb);
      ya0[j] = pk_bf16(asf(c << 16) * rsd, asf(c & 0xffff0000u) * rsd);
    }
  }
  u32x4 wqt[8];
  tr8<4>(wqr, wqt);
  u32x4 ones = {0x3f803f80u, 0x3f803f80u, 0x3f803f80u, 0x3f803f80u};
  PIN(ones);
  auto none = []() {};
  const f32x4 zero4 = {0.f, 0.f, 0.f, 0.f};

  auto s1_core = [&](u32x4 a, int dst, bool on) {
    f32x4 d[8];
#pragma unroll
    for (int c = 0; c < 8; ++c) d[c] = mfma(a, wqt[c], zero4);
#pragma unroll
    for (int c = 0; c < 8; ++c) PIN(d[c]);
    float keep[4], send[4];
#pragma unroll
    for (int j = 0; j < 4; ++j) send[j] = bit3 ? d[j][1] : d[4 + j][0];
#pragma unroll
    for (int j = 0; j < 4; ++j) keep[j] = bit3 ? d[4 + j][1] : d[j][0];
    SCHED();
    u32x4 o;
#pragma unroll
    for (int j = 0; j < 4; ++j) { float t = keep[j] + dpp<ROR8>(send[j]); PIN(t); o[j] = tag8(t, t8v); }
    if (on) stsc1(rs, dst, o);
  };
  if (tid < 4) ((u32*)(s_x1 + 512))[tid] = 0x3f803f80u;
  s1_core(bit3 ? ones : ya0, (kg == 0 ? QP_OFF : CS_OFF) + ((b * 8 + gq) * 8 + s) * 2048 + w * 256 + i16 * 16, (kg & 1) == 0);
  STAMP(1);
  __builtin_amdgcn_sched_barrier(0);
  if (LBAR & 2) asm volatile("s_barrier" ::: "memory");
  __builtin_amdgcn_sched_barrier(0);
  if (LORD == 2) load_v(0, 4);
  if (LORD == 3) load_v(0, 8);
  __builtin_amdgcn_sched_barrier(0);
  if (LBAR & 4) asm volatile("s_barrier" ::: "memory");
  __builtin_amdgcn_sched_barrier(0);
  auto load_wo = [&]() {
    const uint16_t* p = wo + (size_t)(go * 512 + (((l >> 3) & 1) * 4 + kg) * 64 + w) * 512 + s * 64 + (l & 7) * 8;
#pragma unroll
    for (int r = 0; r < 8; ++r) wor[r] = *(const u32x4*)(p + r * 8 * 512);
  };
  auto load_w1 = [&]() {
    const uint16_t* p = w1 + (size_t)(w * 64 + ((l >> 3) & 1) * 32 + kg * 8) * FF + f0 + (l & 7) * 4;
#pragma unroll
    for (int r = 0; r < 8; ++r) w1r[r] = *(const u32x2*)(p + r * FF);
  };
  auto load_w2 = [&]() {
    const uint16_t* p2 = w2 + (size_t)(f0 + kg * 8) * 512 + w * 64 + i16 * 4;
#pragma unroll
    for (int r = 0; r < 8; ++r) w2r[r] = *(const u32x2*)(p2 + r * 512);
  };

  const int xo = X2_OFF + bg * 2048 + w * 256 + c8 * 4;
  u32x4 vt[8], wot[8], w1t[4], w2t[4];
  float csq = 0.f, cs1 = 0.f;
  auto red8 = [&](const float (&v)[8]) {
    const float u0 = rs32(v[0], v[4]), u1 = rs32(v[1], v[5]), u2 = rs32(v[2], v[6]), u3 = rs32(v[3], v[7]);
    return rs8(rs16(u0, u2), rs16(u1, u3), bit3);
  };

#pragma unroll
  for (int layer = 0; layer < 2; ++layer) {
    {
      const int qo = (bg * 8 + (l >> 3)) * 2048 + w * 256 + (l & 7) * 16;
      int od = OP_OFF + ((layer * 32 + bg) * 8 + s) * 2048 + w * 256 + ((l >> 2) & 1) * 128 + (l >> 3) * 16 + (l & 3) * 4;
      PIN(od);
      float a[8], mean = 0.f, rstd = 1.f;
      DLY(layer * 4);
      if (layer == 0) {
        u32x4 v[4];
        const int off[4] = {QP_OFF + qo, QP_OFF + qo + 128, CS_OFF + qo, CS_OFF + qo + 128};
        gather<4>(rs, off, t8, v, [&]() { if (WOPOS == 0) load_wo(); });
        STAMP(2);
        float c[8];
#pragma unroll
        for (int j = 0; j < 4; ++j) { a[j] = asf(v[0][j]); a[4 + j] = asf(v[1][j]); c[j] = asf(v[2][j]); c[4 + j] = asf(v[3][j]); }
        csq = red8(c);
      } else {
        u32x4 v[3];
        const int off[3] = {ST_OFF + (bg * 8 + (l >> 3)) * 128 + (l & 7) * 16, QP_OFF + 256 * 2048 + qo, QP_OFF + 256 * 2048 + qo + 128};
        gather<3>(rs, off, t8, v, [&]() { xres = __builtin_amdgcn_raw_buffer_load_b32(rs, xo, 0, AUX_SC1); });
        STAMP(10);
        CST(0);
#pragma unroll
        for (int j = 0; j < 4; ++j) { a[j] = asf(v[1][j]); a[4 + j] = asf(v[2][j]); }
        float u = rs32(asf(v[0][0]) + asf(v[0][1]), asf(v[0][2]) + asf(v[0][3]));
        u = rs16(u, u);
        u += dpp<ROR8>(u);
        auto r = __builtin_amdgcn_permlane32_swap(asu(u), asu(u), false, false);
        mean = asf(r[0]) * (1.f / 512.f);
        rstd = __builtin_amdgcn_rsqf(fmx(asf(r[1]) * (1.f / 512.f) - mean * mean, 0.f) + EPS);
      }
      const float qv = layer == 0 ? red8(a) : (red8(a) - mean * csq) * rstd;
      s_q[w * 64 + c8] = (uint16_t)(pk_bf16(qv, qv) & 0xffffu);
      CST(1);
      wg_barrier();
      CST(2);
      const u32x4 qf = *(const u32x4*)(s_q + (i16 >> 2) * 128 + kg * 32 + (i16 & 3) * 8);
      f32x4 d[8];
#pragma unroll
      for (int m = 0; m < 8; ++m) d[m] = mfma(kr[m], qf, zero4);
      SCHED();
      float sv[8];
#pragma unroll
      for (int m = 0; m < 8; ++m) { sv[m] = d[m][0] + dpp<0x55>(d[m][1]); PIN(sv[m]); }
#pragma unroll
      for (int m = 0; m < 8; ++m) { sv[m] += dpp<0xAA>(d[m][2]); PIN(sv[m]); }
#pragma unroll
      for (int m = 0; m < 8; ++m) { sv[m] += dpp<0xFF>(d[m][3]); PIN(sv[m]); }
      if (layer == 0) tr8<4>(vr, vt);
      CST(3);
      if (layer == 0) {
#pragma unroll
        for (int j = 0; j < 4; ++j) {
          const u32 c = pk_bf16(sv[2 * j], sv[2 * j + 1]);
          const f32x2 t = f32x2{asf(c << 16), asf(c & 0xffff0000u)} * 0.08838834764831845f;
          const u32 e = pk_bf16(t[0], t[1]);
          sv[2 * j] = asf(e << 16); sv[2 * j + 1] = asf(e & 0xffff0000u);
        }
      }
      const float sc = (layer == 0 ? 1.f : 0.08838834764831845f) * 1.4426950408889634f;
      float m = fmx(fmx(fmx(fmx(sv[0], sv[1]), sv[2]), fmx(fmx(sv[3], sv[4]), sv[5])), fmx(sv[6], sv[7]));
      m = mx32(mx16(m)) * sc;
      u32x4 pa;
#pragma unroll
      for (int j = 0; j < 4; ++j) {
        const f32x2 e = f32x2{sv[2 * j], sv[2 * j + 1]} * sc - m;
        pa[j] = pk_bf16(__builtin_amdgcn_exp2f(e[0]), __builtin_amdgcn_exp2f(e[1]));
      }
      CST(4);
      f32x4 dl = mfma(pa, ones, zero4);
      SCHED();
#pragma unroll
      for (int c2 = 0; c2 < 8; ++c2) d[c2] = mfma(pa, vt[c2], zero4);
      PIN(dl);
#pragma unroll
      for (int c2 = 0; c2 < 8; ++c2) PIN(d[c2]);
      if (i16 == 4 * kg) { s_ml[kg * 16 + w] = m; s_ml[kg * 16 + 8 + w] = dl[0]; }
#pragma unroll
      for (int c2 = 0; c2 < 8; ++c2) s_ow[(w * 8 + c2) * 64 + l] = d[c2][0];
      CST(5);
      wg_barrier();
      CST(6);
      const f32x4 m0 = *(const f32x4*)(s_ml + kg * 16), m1 = *(const f32x4*)(s_ml + kg * 16 + 4);
      const f32x4 l0 = *(const f32x4*)(s_ml + kg * 16 + 8), l1 = *(const f32x4*)(s_ml + kg * 16 + 12);
      float ov[8];
#pragma unroll
      for (int u = 0; u < 8; ++u) ov[u] = s_ow[(u * 8 + w) * 64 + l];
      const float M = fmx(fmx(fmx(fmx(m0[0], m0[1]), m0[2]), fmx(fmx(m0[3], m1[0]), m1[1])), fmx(m1[2], m1[3]));
      const f32x2 mp[4] = {lo2(m0), hi2(m0), lo2(m1), hi2(m1)}, lp[4] = {lo2(l0), hi2(l0), lo2(l1), hi2(l1)};
      f32x2 L2, o2;
#pragma unroll
      for (int u = 0; u < 4; ++u) {
        const f32x2 e = mp[u] - M;
        const f32x2 c = {__builtin_amdgcn_exp2f(e[0]), __builtin_amdgcn_exp2f(e[1])};
        const f32x2 ou = {ov[2 * u], ov[2 * u + 1]};
        L2 = u ? c * lp[u] + L2 : c * lp[u];
        o2 = u ? c * ou + o2 : c * ou;
      }
      PIN(L2); PIN(o2);
      const float L = L2[0] + L2[1];
      const float on = (o2[0] + o2[1]) * __builtin_amdgcn_rcpf(L);
      const float lw = M + __builtin_amdgcn_logf(L);
      __builtin_amdgcn_raw_buffer_store_b32(__builtin_amdgcn_perm(pk_bf16(on, on), asu(lw), 0x0706000cu | ((l & 3) << 8)) | t8, rs, od, 0, AUX_SC1);
      STAMP(3 + layer * 8);
      CST(7);
    }
    {
      if (layer == 0 && WOPOS == 1) { SCHED(); load_wo(); SCHED(); }
      const int oo = OP_OFF + ((layer * 32 + b * 8 + go) * 8 + (l & 7)) * 2048 + w * 256 + (((l >> 3) & 1) * 4 + kg) * 16;
      const int off[2] = {oo, oo + 128};
      int wd = WP_OFF + (layer * 4 + b) * 16384 + g * 2048 + s * 256 + (l & 1) * 128 + (i16 >> 1) * 16 + (l >> 5) * 8 + (kg & 1) * 4;
      PIN(wd);
      u32x4 v[2];
      DLY(1 + layer * 4);
      if (layer == 0) gather<2, false>(rs, off, t8, v, [&]() {
        if (W1POS == 0) { load_w1(); SCHED(); }
        tr8<4>(wor, wot);
#pragma unroll
        for (int c = 0; c < 8; ++c) PIN(wot[c]);
      }); else gather<2, false>(rs, off, t8, v, [&]() {
        if (__builtin_expect(!__all(((xres ^ t16) & 0xffffu) == 0), 0)) {
          for (u32 spins = 0; spins < SPIN_CAP; ++spins) {
            asm volatile("" ::: "memory");
            xres = __builtin_amdgcn_raw_buffer_load_b32(rs, xo, 0, AUX_SC1);
            if (__all(((xres ^ t16) & 0xffffu) == 0)) break;
          }
        }
        xres &= 0xffff0000u;
        PIN(xres);
      });
      STAMP(4 + layer * 8);
      CST(8);
      const float lw = asf(__builtin_amdgcn_perm(__builtin_amdgcn_perm(v[0][3], v[0][2], 0x05010000u), __builtin_amdgcn_perm(v[0][1], v[0][0], 0x00000501u), 0x07060100u));
      float M = fmx(lw, dpp<QX1>(lw)); M = fmx(M, dpp<QX2>(M)); M = fmx(M, dpp<HMIR>(M));
      float cw = __builtin_amdgcn_exp2f(lw - M);
      float T = cw + dpp<QX1>(cw); T += dpp<QX2>(T); T += dpp<HMIR>(T);
      cw *= __builtin_amdgcn_rcpf(T);
      u32x4 a;
#pragma unroll
      for (int j = 0; j < 4; ++j)
        a[j] = pk_bf16(asf(v[j >> 1][(j & 1) * 2] & 0xffff0000u) * cw, asf(v[j >> 1][(j & 1) * 2 + 1] & 0xffff0000u) * cw);
      f32x4 d[8];
#pragma unroll
      for (int c = 0; c < 8; ++c) d[c] = mfma(a, wot[c], zero4);
      f32x4 z[2];
#pragma unroll
      for (int c = 0; c < 8; ++c) { f32x2 t = lo2(d[c]) + hi2(d[c]); PIN(t); float r = t[0] + t[1]; PIN(r); z[c >> 2][c & 3] = r; }
      if (((l >> 3) & 1) == (l >> 5)) {
        *(f32x4*)(s_wp + (w * 4 + kg) * 64 + (l & 7) * 8) = z[0];
        *(f32x4*)(s_wp + (w * 4 + kg) * 64 + (l & 7) * 8 + 4) = z[1];
      }
      CST(9);
      wg_barrier();
      CST(10);
      if (w == 0) {
        f32x4 p[8];
#pragma unroll
        for (int k = 0; k < 8; ++k) p[k] = *(const f32x4*)(s_wp + (kg * 8 + k) * 64 + i16 * 4);
        const f32x4 acc = ((p[0] + p[1]) + (p[2] + p[3])) + ((p[4] + p[5]) + (p[6] + p[7]));
        const float t = rs16(rs32(acc[0], acc[2]), rs32(acc[1], acc[3]));
        __builtin_amdgcn_raw_buffer_store_b32(tag8(t, t8v), rs, wd, 0, AUX_SC1);
      }
      STAMP(5 + layer * 8);
      CST(11);
    }
    {
      if (layer == 0 && W1POS == 1) { SCHED(); load_w1(); SCHED(); }
      const int wo_ = WP_OFF + (layer * 4 + b) * 16384 + (l >> 3) * 2048 + w * 256 + (l & 7) * 16;
      const int off[2] = {wo_, wo_ + 128};
      int md = MP_OFF + (((layer * 4 + b) * 8 + w) * 56 + g * 7 + s) * 256 + l * 16;
      PIN(md);
      u32x4 v[2];
      DLY(2 + layer * 4);
      if (layer == 0) gather<2>(rs, off, t8, v, [&]() {
        load_w2();
        SCHED();
        tr8<2>(w1r, w1t);
#pragma unroll
        for (int c = 0; c < 4; ++c) PIN(w1t[c]);
      }); else gather<2>(rs, off, t8, v, none);
      STAMP(6 + layer * 8);
      CST(12);
      float y[8];
#pragma unroll
      for (int j = 0; j < 8; ++j) y[j] = asf(v[j >> 2][j & 3]);
      const float x1 = rbf(red8(y)) + asf(xres);
      s_x1[w * 64 + c8] = (uint16_t)(pk_bf16(x1, x1) & 0xffffu);
      asm volatile("" ::: "memory");
      const int xf = w * 64 + (i16 & 1) * 32 + kg * 8;
      const u32x4 am = *(const u32x4*)(s_x1 + (bit3 ? 512 : xf));
      const u32x4 bx = *(const u32x4*)(s_x1 + xf);
      f32x4 d[4];
#pragma unroll
      for (int c = 0; c < 4; ++c) d[c] = mfma(am, w1t[c], zero4);
      f32x4 gr = mfma(am, bx, zero4);
#pragma unroll
      for (int c = 0; c < 4; ++c) PIN(d[c]);
      if (l < 16 || (layer == 0 && kg == 2)) {
        const f32x4 hv = bit3 ? f32x4{d[0][1], d[1][1], d[2][1], d[3][1]} : f32x4{d[0][0], d[1][0], d[2][0], d[3][0]};
        *(f32x4*)(s_hp + (l >> 5) * 512 + (w * 2 + ((l >> 3) & 1)) * 32 + (l & 7) * 4) = hv;
      }
      if ((l & 30) == 0) s_st[(l >> 5) * 16 + w * 2 + (l & 1)] = (l & 1) ? gr[1] : gr[0];
      if (layer == 0) tr8<2>(w2r, w2t);
      wg_barrier();
      if (mlp_on) {
        float st = s_st[l & 31];
        float hp8[8];
#pragma unroll
        for (int p = 0; p < 8; ++p) hp8[p] = s_hp[((l >> 5) * 8 + p) * 32 + (l & 31)];
        if (layer == 0) {
          float ac = 0.f;
#pragma unroll
          for (int p = 0; p < 8; ++p) ac += s_hp[512 + ((l >> 5) * 8 + p) * 32 + (l & 31)];
          cs1 = rs32(ac, ac);
        }
        st += dpp<ROR8>(st); st += dpp<ROR4>(st); st += dpp<ROR2>(st); st += dpp<ROR1>(st);
        float acc = sum8(hp8);
        acc = rs32(acc, acc);
        auto r = __builtin_amdgcn_permlane16_swap(asu(st), asu(st), false, false);
        const float mu = asf(r[1]) * (1.f / 512.f);
        const float var = fmx(asf(r[0]) * (1.f / 512.f) - mu * mu, 0.f);
        const float r12 = __builtin_amdgcn_rsqf(var + EPS * (var + EPS));
        const float hp = (acc - mu * cs1) * r12 + b1v;
        const float u = hp * (-2.302208198144325f - 0.1029432395800235f * (hp * hp));
        const float hv = hp * __builtin_amdgcn_rcpf(1.f + __builtin_amdgcn_exp2f(u));
        s_a[w * 64 + l] = (uint16_t)(pk_bf16(hv, hv) & 0xffffu);
        asm volatile("" ::: "memory");
        const u32x4 ha = *(const u32x4*)(s_a + w * 64 + kg * 8);
        f32x4 e[4];
#pragma unroll
        for (int c = 0; c < 4; ++c) e[c] = mfma(ha, w2t[c], zero4);
#pragma unroll
        for (int c = 0; c < 4; ++c) PIN(e[c]);
        const u32x4 o = {tag8(e[0][0], t8v), tag8(e[1][0], t8v), tag8(e[2][0], t8v), tag8(e[3][0], t8v)};
        if (l < 16) stsc1(rs, md, o);
      }
      STAMP(7 + layer * 8);
      CST(13);
    }
    if (layer == 0) {
      float xr = asf((u32)s_x1[s * 64 + l] << 16);
      PIN(xr);
      const int mo = MP_OFF + ((b * 8 + s) * 56 + w * 7 + kg) * 256 + i16 * 16;
      const int off[2] = {mo, kg < 3 ? mo + 1024 : mo};
      float two = kg < 3 ? 1.f : 0.f;
      int qd = QP_OFF + ((32 + b * 8 + gq) * 8 + s) * 2048 + w * 256 + i16 * 16;
      int sd = ST_OFF + ((b * 8 + gq) * 8 + s) * 128 + (l & 7) * 16 + (l >> 5) * 8;
      int xpi = w * 64 + i16 * 4 + (l >> 5) * 2 + (kg & 1), sai = w * 64 + (i16 & 1) * 32 + kg * 8;
      float b2p = b2a;
      PIN(two); PIN(qd); PIN(sd); PIN(xpi); PIN(sai); PIN(b2p);
      u32x4 v[2];
      DLY(3);
      gather<2>(rs, off, t8, v, none);
      STAMP(8);
      float y[8];
#pragma unroll
      for (int j = 0; j < 4; ++j) y[j] = asf(v[0][j]) + two * asf(v[1][j]);
      s_xp[xpi] = rs16(rs32(y[0], y[2]), rs32(y[1], y[3]));
      wg_barrier();
#pragma unroll
      for (int u = 0; u < 8; ++u) y[u] = s_xp[u * 64 + l];
      const float x2f = rbf(rbf(sum8(y)) + b2p) + xr;
      const u32 x2 = pk_bf16(x2f, x2f);
      s_a[w * 64 + l] = (uint16_t)(x2 & 0xffffu);
      asm volatile("" ::: "memory");
      const u32x4 a = *(const u32x4*)(s_a + sai);
      if (w == 6) {
        const float xv = asf(x2 & 0xffff0000u);
        float u = rs32(xv, xv * xv);
        u = rs16(u, u);
        u += dpp<ROR8>(u); u += dpp<ROR4>(u); u += dpp<ROR2>(u); u += dpp<ROR1>(u);
        const float hi = asf(asu(u) & 0xffffff00u);
        const u32x2 o = {asu(hi) | t8, tag8(u - hi, t8v)};
        if ((l & 24) == 0) __builtin_amdgcn_raw_buffer_store_b64(o, rs, sd, 0, AUX_SC1);
      }
      s1_core(a, qd, l < 16);
      if (w == 7) __builtin_amdgcn_raw_buffer_store_b32((x2 & 0xffff0000u) | t16, rs, X2_OFF + (b * 8 + gq) * 2048 + s * 256 + l * 4, 0, AUX_SC1);
      STAMP(9);
    } else if (w == 0) {
      const int oc = s * 64 + g * 8 + e8;
      const float xr = asf((u32)s_x1[oc] << 16);
      const int mo = MP_OFF + (((4 + b) * 8 + s) * 56 + (l < 56 ? l : 0)) * 256 + g * 32;
      const int off[2] = {mo, mo + 16};
      u32x4 v[2];
      DLY(7);
      gather<2>(rs, off, t8, v, none);
      STAMP(16);
      float y[8];
#pragma unroll
      for (int j = 0; j < 8; ++j) y[j] = l < 56 ? asf(v[j >> 2][j & 3]) : 0.f;
      float t = red8(y);
      t += dpp<QX1>(t); t += dpp<QX2>(t); t += dpp<HMIR>(t);
      const float r = rbf(rbf(t) + b2f) + xr;
      if ((l & 7) == 0) out[b * 512 + oc] = (uint16_t)(pk_bf16(r, r) & 0xffffu);
      if (l == 0 && g == 0 && s == 0) __builtin_amdgcn_raw_buffer_store_b32(tn, rs, EP_OFF + b * 256, 0, AUX_SC1);
      STAMP(17);
    }
  }
}

extern "C" void kernel_launch(void* const* d_in, const int* in_sizes, int n_in, void* d_out, int out_size, void* d_ws,
                              size_t ws_size, hipStream_t stream) {
  (void)in_sizes; (void)n_in; (void)out_size; (void)ws_size;
  fused_decode<<<256, 512, 0, stream>>>((const uint16_t*)d_in[0], (const uint16_t*)d_in[1], (const uint16_t*)d_in[2],
                                        (const uint16_t*)d_in[3], (const uint16_t*)d_in[4], (const uint16_t*)d_in[6],
                                        (const uint16_t*)d_in[7], (const uint16_t*)d_in[8], (const uint16_t*)d_in[9],
                                        (uint16_t*)d_out, (char*)d_ws TUNE_ARG);
}
```

```cpp
#include <hip/hip_runtime.h>
#include <hip/hip_bf16.h>
#include <stdint.h>

#define DIAG 0

typedef unsigned int u32;
typedef __attribute__((ext_vector_type(4))) unsigned int u32x4;
typedef __attribute__((ext_vector_type(2))) unsigned int u32x2;
typedef __attribute__((ext_vector_type(4))) float f32x4;
typedef __attribute__((ext_vector_type(2))) float f32x2;
typedef __attribute__((ext_vector_type(8))) __bf16 bf16x8;
typedef __attribute__((ext_vector_type(2))) __bf16 bf16x2;

namespace {
constexpr int SK = 8192, WIN = 2048, FF = 1792;
constexpr float EPS = 1e-5f;
constexpr u32 SPIN_CAP = 1u << 17;
constexpr int EP_OFF = 0;
constexpr int QP_OFF = 4096;
constexpr int CS_OFF = QP_OFF + 2 * 256 * 2048;
constexpr int X2_OFF = CS_OFF + 256 * 2048;
constexpr int OP_OFF = X2_OFF + 32 * 2048;
constexpr int ST_OFF = OP_OFF + 2 * 256 * 2048;
constexpr int WP_OFF = ST_OFF + 256 * 128;
constexpr int MP_OFF = WP_OFF + 2 * 4 * 16384;
constexpr int WS_END = MP_OFF + 2 * 4 * 8 * 56 * 256;
constexpr int DG_OFF = WS_END;
constexpr int AUX_SC1 = 16;

__device__ __forceinline__ float asf(u32 x) { return __builtin_bit_cast(float, x); }
__device__ __forceinline__ u32 asu(float x) { return __builtin_bit_cast(u32, x); }
__device__ __forceinline__ u32 pk_bf16(float lo, float hi) {
  f32x2 v = {lo, hi};
  return __builtin_bit_cast(u32, __builtin_convertvector(v, bf16x2));
}
__device__ __forceinline__ float rbf(float x) {
  return asf(pk_bf16(x, x) & 0xffff0000u);
}
__device__ __forceinline__ float fmx(float a, float b) { return __builtin_elementwise_maximum(a, b); }
#define PIN(x) asm volatile("" : "+v"(x))
#define SCHED() __builtin_amdgcn_sched_barrier(0)
template <int CTRL> __device__ __forceinline__ float dpp(float x) {
  return asf(__builtin_amdgcn_update_dpp(0, asu(x), CTRL, 0xf, 0xf, true));
}
constexpr int ROR8 = 0x128, ROR4 = 0x124, ROR2 = 0x122, ROR1 = 0x121, QX1 = 0xB1, QX2 = 0x4E, HMIR = 0x141;
__device__ __forceinline__ float rs32(float a, float b) {
  auto r = __builtin_amdgcn_permlane32_swap(asu(a), asu(b), false, false);
  return asf(r[0]) + asf(r[1]);
}
__device__ __forceinline__ float rs16(float a, float b) {
  auto r = __builtin_amdgcn_permlane16_swap(asu(a), asu(b), false, false);
  return asf(r[0]) + asf(r[1]);
}
__device__ __forceinline__ float rs8(float a, float b, bool bit3) {
  return (bit3 ? b : a) + dpp<ROR8>(bit3 ? a : b);
}
__device__ __forceinline__ float mx32(float a) {
  auto r = __builtin_amdgcn_permlane32_swap(asu(a), asu(a), false, false);
  return fmx(asf(r[0]), asf(r[1]));
}
__device__ __forceinline__ float mx16(float a) {
  auto r = __builtin_amdgcn_permlane16_swap(asu(a), asu(a), false, false);
  return fmx(asf(r[0]), asf(r[1]));
}
__device__ __forceinline__ float wave_sum(float a) {
  a += dpp<ROR8>(a); a += dpp<ROR4>(a); a += dpp<ROR2>(a); a += dpp<ROR1>(a);
  a = rs16(a, a); return rs32(a, a);
}
__device__ __forceinline__ f32x4 mfma(u32x4 a, u32x4 b, f32x4 c) {
  return __builtin_amdgcn_mfma_f32_16x16x32_bf16(__builtin_bit_cast(bf16x8, a), __builtin_bit_cast(bf16x8, b), c, 0, 0, 0);
}
template <int NW, typename V> __device__ __forceinline__ void tr8(const V (&r)[8], u32x4 (&t)[2 * NW]) {
#pragma unroll
  for (int c = 0; c < NW; ++c)
#pragma unroll
    for (int j = 0; j < 4; ++j) {
      t[2 * c][j] = __builtin_amdgcn_perm(r[2 * j + 1][c], r[2 * j][c], 0x05040100u);
      t[2 * c + 1][j] = __builtin_amdgcn_perm(r[2 * j + 1][c], r[2 * j][c], 0x07060302u);
    }
}
__device__ __forceinline__ u32x4 ldsc1(__amdgpu_buffer_rsrc_t rs, int off) {
  return __builtin_amdgcn_raw_buffer_load_b128(rs, off, 0, AUX_SC1);
}
__device__ __forceinline__ void stsc1(__amdgpu_buffer_rsrc_t rs, int off, u32x4 v) {
  __builtin_amdgcn_raw_buffer_store_b128(v, rs, off, 0, AUX_SC1);
}
__device__ __forceinline__ u32 tag8(float v, u32 t8) { return (asu(v) & 0xffffff00u) | t8; }
__device__ __forceinline__ u32 or4(u32x4 v) { return (v[0] | v[1]) | (v[2] | v[3]); }
__device__ __forceinline__ void wg_barrier() {
  asm volatile("s_waitcnt lgkmcnt(0)\n\ts_barrier" ::: "memory");
}
template <int N, bool CLEAN> __device__ __forceinline__ bool untag(u32x4 (&v)[N], u32 tg) {
  u32 bad = 0;
#pragma unroll
  for (int k = 0; k < N; ++k) {
    if (CLEAN) { v[k] ^= tg; bad |= or4(v[k]); }
    else bad |= __builtin_amdgcn_bitop3_b32(v[k][0], v[k][1], tg, 0x7e) | __builtin_amdgcn_bitop3_b32(v[k][2], v[k][3], tg, 0x7e);
  }
  return __all((bad & 0xffu) == 0);
}
template <int N, bool CLEAN = true, typename F>
__device__ __forceinline__ void gather(__amdgpu_buffer_rsrc_t rs, const int (&off)[N], u32 tg, u32x4 (&v)[N], F&& behind) {
#pragma unroll
  for (int k = 0; k < N; ++k) v[k] = ldsc1(rs, off[k]);
  SCHED();
  behind();
  SCHED();
  if (__builtin_expect(!untag<N, CLEAN>(v, tg), 0)) {
    for (u32 spins = 0; spins < SPIN_CAP; ++spins) {
      asm volatile("" ::: "memory");
#pragma unroll
      for (int k = 0; k < N; ++k) v[k] = ldsc1(rs, off[k]);
      if (untag<N, CLEAN>(v, tg)) break;
    }
  }
}
__device__ __forceinline__ f32x2 lo2(f32x4 v) { return __builtin_shufflevector(v, v, 0, 1); }
__device__ __forceinline__ f32x2 hi2(f32x4 v) { return __builtin_shufflevector(v, v, 2, 3); }
__device__ __forceinline__ float sum8(const float (&y)[8]) {
  f32x2 t = (f32x2{y[0], y[1]} + f32x2{y[2], y[3]}) + (f32x2{y[4], y[5]} + f32x2{y[6], y[7]});
  PIN(t);
  return t[0] + t[1];
}
}

#define STAMP(i) do {} while (0)
#define CST(i) do {} while (0)
#define KDELAY {0, 18, 18, 18, 18, 18, 18, 18}
constexpr int kDelay[8] = KDELAY;
#define DLY(i) do { if (kDelay[i] > 0) asm volatile("s_sleep %0" :: "i"(kDelay[i])); } while (0)
#define TUNE_PARAM
#define TUNE_ARG
#define QSHIFT 1
#define LORD 3
#define LBAR 7
#define TEST 0
#define WOPOS 0
#define W1POS 1
#define WARM 2
#define OSHIFT 1

__global__ void __launch_bounds__(512, 2)
fused_decode(const uint16_t* __restrict__ x, const uint16_t* __restrict__ kc, const uint16_t* __restrict__ vc,
             const uint16_t* __restrict__ wq, const uint16_t* __restrict__ wo, const uint16_t* __restrict__ w1,
             const uint16_t* __restrict__ b1, const uint16_t* __restrict__ w2, const uint16_t* __restrict__ b2,
             uint16_t* __restrict__ out, char* ws TUNE_PARAM) {
  __shared__ __attribute__((aligned(16))) float s_ow[64 * 64];
  __shared__ __attribute__((aligned(16))) float s_ml[4 * 16];
  __shared__ __attribute__((aligned(16))) uint16_t s_q[512];
  __shared__ __attribute__((aligned(16))) uint16_t s_x1[512 + 8];
  __shared__ __attribute__((aligned(16))) float s_hp[2 * 16 * 32];
  __shared__ __attribute__((aligned(16))) float s_st[32];
  __shared__ __attribute__((aligned(16))) uint16_t s_a[8 * 64];
  __shared__ __attribute__((aligned(16))) float s_xp[8 * 64];
  __shared__ __attribute__((aligned(16))) float s_wp[32 * 64];

  const int tid = threadIdx.x, bid = blockIdx.x;
  const int w = __builtin_amdgcn_readfirstlane(tid >> 6);
  const int l = tid & 63, i16 = l & 15, kg = l >> 4;
  const int g = bid & 7, s = (bid >> 3) & 7, b = bid >> 6;
  const int bg = b * 8 + g;
  const bool bit3 = (l & 8) != 0;
  const bool mlp_on = s < 7;
  const int f0 = g * 224 + (mlp_on ? s : 6) * 32;
  const __amdgpu_buffer_rsrc_t rs = __builtin_amdgcn_make_buffer_rsrc((void*)ws, 0, WS_END, 0x00020000);
  STAMP(0);
  const int gq = (g + QSHIFT) & 7, go = (g + OSHIFT) & 7;

  const u32 ep = __builtin_amdgcn_raw_buffer_load_b32(rs, EP_OFF + b * 256, 0, AUX_SC1);
  const u32x4 xfull = *(const u32x4*)(x + b * 512 + l * 8);
  const u32x4 xa0 = *(const u32x4*)(x + b * 512 + s * 64 + (i16 & 1) * 32 + kg * 8);
  const int e8 = (l >> 5) * 4 + ((l >> 4) & 1) * 2 + ((l >> 3) & 1);
  const int c8 = (l & 7) * 8 + e8;
  u32x4 wqr[8], kr[8], vr[8], wor[8];
  u32x2 w1r[8], w2r[8];
  {
    const uint16_t* p = wq + (size_t)(s * 64 + ((l >> 3) & 1) * 32 + kg * 8) * 4096 + gq * 512 + w * 64 + (l & 7) * 8;
#pragma unroll
    for (int r = 0; r < 8; ++r) wqr[r] = *(const u32x4*)(p + r * 4096);
  }
  u32 warm = 0;
  if (w == 6) {
    const char* pc = (const char*)__builtin_amdgcn_s_getpc();
    if (l < 8) warm = *(const volatile u32*)((((size_t)pc) & ~(size_t)127) + ((bid >> 3) * 8 + l) % 100 * 128);
  }
  __builtin_amdgcn_sched_barrier(0);
  asm volatile("s_barrier" ::: "memory");
  __builtin_amdgcn_sched_barrier(0);
  const size_t kvbase = ((size_t)bg * SK + (SK - WIN) + s * 256 + w * 32) * 128;
  {
    const uint16_t* p = kc + kvbase + (i16 >> 2) * 8 * 128 + kg * 32 + (i16 & 3) * 8;
#pragma unroll
    for (int m = 0; m < 8; ++m) kr[m] = __builtin_nontemporal_load((const u32x4*)(p + m * 128));
  }
  auto load_v = [&](int r0, int r1) {
    const uint16_t* p = vc + kvbase + kg * 8 * 128 + i16 * 8;
#pragma unroll
    for (int r = r0; r < r1; ++r) vr[r] = __builtin_nontemporal_load((const u32x4*)(p + r * 128));
  };
  if (LORD == 0) load_v(0, 8);
  __builtin_amdgcn_sched_barrier(0);
  if (LBAR & 1) asm volatile("s_barrier" ::: "memory");
  const float b1v = asf((u32)b1[f0 + (l & 31)] << 16);
  const float b2a = asf((u32)b2[s * 64 + l] << 16);
  const float b2f = asf((u32)b2[s * 64 + g * 8 + e8] << 16);
  u32 xres = (u32)x[b * 512 + w * 64 + c8] << 16;
  __builtin_amdgcn_sched_barrier(0);
  const u32 tn = __builtin_amdgcn_readfirstlane(ep) + 1u;
  const u32 t8 = tn & 0xffu, t16 = tn & 0xffffu;
  u32 t8v = t8;
  PIN(t8v);

  u32x4 ya0;
  {
    float xv[8];
#pragma unroll
    for (int j = 0; j < 4; ++j) { xv[2 * j] = asf(xfull[j] << 16); xv[2 * j + 1] = asf(xfull[j] & 0xffff0000u); }
    float sm = 0.f;
#pragma unroll
    for (int j = 0; j < 8; ++j) sm += xv[j];
    const float mean = wave_sum(sm) * (1.f / 512.f);
    float sq = 0.f;
#pragma unroll
    for (int j = 0; j < 8; ++j) sq += (xv[j] - mean) * (xv[j] - mean);
    const float mb = rbf(mean);
    const float rsd = __builtin_amdgcn_rcpf(rbf(__builtin_amdgcn_sqrtf(rbf(rbf(wave_sum(sq) * (1.f / 512.f)) + EPS))));
#pragma unroll
    for (int j = 0; j < 4; ++j) {
      const u32 c = pk_bf16(asf(xa0[j] << 16) - mb, asf(xa0[j] & 0xffff0000u) - mb);
      ya0[j] = pk_bf16(asf(c << 16) * rsd, asf(c & 0xffff0000u) * rsd);
    }
  }
  u32x4 wqt[8];
  tr8<4>(wqr, wqt);
  u32x4 ones = {0x3f803f80u, 0x3f803f80u, 0x3f803f80u, 0x3f803f80u};
  PIN(ones);
  auto none = []() {};
  const f32x4 zero4 = {0.f, 0.f, 0.f, 0.f};

  auto s1_core = [&](u32x4 a, int dst, bool on) {
    f32x4 d[8];
#pragma unroll
    for (int c = 0; c < 8; ++c) d[c] = mfma(a, wqt[c], zero4);
#pragma unroll
    for (int c = 0; c < 8; ++c) PIN(d[c]);
    float keep[4], send[4];
#pragma unroll
    for (int j = 0; j < 4; ++j) send[j] = bit3 ? d[j][1] : d[4 + j][0];
#pragma unroll
    for (int j = 0; j < 4; ++j) keep[j] = bit3 ? d[4 + j][1] : d[j][0];
    SCHED();
    u32x4 o;
#pragma unroll
    for (int j = 0; j < 4; ++j) { float t = keep[j] + dpp<ROR8>(send[j]); PIN(t); o[j] = tag8(t, t8v); }
    if (on) stsc1(rs, dst, o);
  };
  if (tid < 4) ((u32*)(s_x1 + 512))[tid] = 0x3f803f80u;
  s1_core(bit3 ? ones : ya0, (kg == 0 ? QP_OFF : CS_OFF) + ((b * 8 + gq) * 8 + s) * 2048 + w * 256 + i16 * 16, (kg & 1) == 0);
  STAMP(1);
  __builtin_amdgcn_sched_barrier(0);
  if (LBAR & 2) asm volatile("s_barrier" ::: "memory");
  __builtin_amdgcn_sched_barrier(0);
  if (LORD == 2) load_v(0, 4);
  if (LORD == 3) load_v(0, 8);
  __builtin_amdgcn_sched_barrier(0);
  if (LBAR & 4) asm volatile("s_barrier" ::: "memory");
  __builtin_amdgcn_sched_barrier(0);
  auto load_wo = [&]() {
    const uint16_t* p = wo + (size_t)(go * 512 + (((l >> 3) & 1) * 4 + kg) * 64 + w) * 512 + s * 64 + (l & 7) * 8;
#pragma unroll
    for (int r = 0; r < 8; ++r) wor[r] = *(const u32x4*)(p + r * 8 * 512);
  };
  auto load_w1 = [&]() {
    const uint16_t* p = w1 + (size_t)(w * 64 + ((l >> 3) & 1) * 32 + kg * 8) * FF + f0 + (l & 7) * 4;
#pragma unroll
    for (int r = 0; r < 8; ++r) w1r[r] = *(const u32x2*)(p + r * FF);
  };
  auto load_w2 = [&]() {
    const uint16_t* p2 = w2 + (size_t)(f0 + kg * 8) * 512 + w * 64 + i16 * 4;
#pragma unroll
    for (int r = 0; r < 8; ++r) w2r[r] = *(const u32x2*)(p2 + r * 512);
  };

  asm volatile("" :: "v"(warm));
  const int xo = X2_OFF + bg * 2048 + w * 256 + c8 * 4;
  u32x4 vt[8], wot[8], w1t[4], w2t[4];
  float csq = 0.f, cs1 = 0.f;
  auto red8 = [&](const float (&v)[8]) {
    const float u0 = rs32(v[0], v[4]), u1 = rs32(v[1], v[5]), u2 = rs32(v[2], v[6]), u3 = rs32(v[3], v[7]);
    return rs8(rs16(u0, u2), rs16(u1, u3), bit3);
  };

#pragma unroll
  for (int layer = 0; layer < 2; ++layer) {
    {
      const int qo = (bg * 8 + (l >> 3)) * 2048 + w * 256 + (l & 7) * 16;
      int od = OP_OFF + ((layer * 32 + bg) * 8 + s) * 2048 + w * 256 + ((l >> 2) & 1) * 128 + (l >> 3) * 16 + (l & 3) * 4;
      PIN(od);
      float a[8], mean = 0.f, rstd = 1.f;
      DLY(layer * 4);
      if (layer == 0) {
        u32x4 v[4];
        const int off[4] = {QP_OFF + qo, QP_OFF + qo + 128, CS_OFF + qo, CS_OFF + qo + 128};
        gather<4>(rs, off, t8, v, [&]() { if (WOPOS == 0) load_wo(); });
        STAMP(2);
        float c[8];
#pragma unroll
        for (int j = 0; j < 4; ++j) { a[j] = asf(v[0][j]); a[4 + j] = asf(v[1][j]); c[j] = asf(v[2][j]); c[4 + j] = asf(v[3][j]); }
        csq = red8(c);
      } else {
        u32x4 v[3];
        const int off[3] = {ST_OFF + (bg * 8 + (l >> 3)) * 128 + (l & 7) * 16, QP_OFF + 256 * 2048 + qo, QP_OFF + 256 * 2048 + qo + 128};
        gather<3>(rs, off, t8, v, [&]() { xres = __builtin_amdgcn_raw_buffer_load_b32(rs, xo, 0, AUX_SC1); });
        STAMP(10);
        CST(0);
#pragma unroll
        for (int j = 0; j < 4; ++j) { a[j] = asf(v[1][j]); a[4 + j] = asf(v[2][j]); }
        float u = rs32(asf(v[0][0]) + asf(v[0][1]), asf(v[0][2]) + asf(v[0][3]));
        u = rs16(u, u);
        u += dpp<ROR8>(u);
        auto r = __builtin_amdgcn_permlane32_swap(asu(u), asu(u), false, false);
        mean = asf(r[0]) * (1.f / 512.f);
        rstd = __builtin_amdgcn_rsqf(fmx(asf(r[1]) * (1.f / 512.f) - mean * mean, 0.f) + EPS);
      }
      const float qv = layer == 0 ? red8(a) : (red8(a) - mean * csq) * rstd;
      s_q[w * 64 + c8] = (uint16_t)(pk_bf16(qv, qv) & 0xffffu);
      CST(1);
      wg_barrier();
      CST(2);
      const u32x4 qf = *(const u32x4*)(s_q + (i16 >> 2) * 128 + kg * 32 + (i16 & 3) * 8);
      f32x4 d[8];
#pragma unroll
      for (int m = 0; m < 8; ++m) d[m] = mfma(kr[m], qf, zero4);
      SCHED();
      float sv[8];
#pragma unroll
      for (int m = 0; m < 8; ++m) { sv[m] = d[m][0] + dpp<0x55>(d[m][1]); PIN(sv[m]); }
#pragma unroll
      for (int m = 0; m < 8; ++m) { sv[m] += dpp<0xAA>(d[m][2]); PIN(sv[m]); }
#pragma unroll
      for (int m = 0; m < 8; ++m) { sv[m] += dpp<0xFF>(d[m][3]); PIN(sv[m]); }
      if (layer == 0) tr8<4>(vr, vt);
      CST(3);
      if (layer == 0) {
#pragma unroll
        for (int j = 0; j < 4; ++j) {
          const u32 c = pk_bf16(sv[2 * j], sv[2 * j + 1]);
          const f32x2 t = f32x2{asf(c << 16), asf(c & 0xffff0000u)} * 0.08838834764831845f;
          const u32 e = pk_bf16(t[0], t[1]);
          sv[2 * j] = asf(e << 16); sv[2 * j + 1] = asf(e & 0xffff0000u);
        }
      }
      const float sc = (layer == 0 ? 1.f : 0.08838834764831845f) * 1.4426950408889634f;
      float m = fmx(fmx(fmx(fmx(sv[0], sv[1]), sv[2]), fmx(fmx(sv[3], sv[4]), sv[5])), fmx(sv[6], sv[7]));
      m = mx32(mx16(m)) * sc;
      u32x4 pa;
#pragma unroll
      for (int j = 0; j < 4; ++j) {
        const f32x2 e = f32x2{sv[2 * j], sv[2 * j + 1]} * sc - m;
        pa[j] = pk_bf16(__builtin_amdgcn_exp2f(e[0]), __builtin_amdgcn_exp2f(e[1]));
      }
      CST(4);
      f32x4 dl = mfma(pa, ones, zero4);
      SCHED();
#pragma unroll
      for (int c2 = 0; c2 < 8; ++c2) d[c2] = mfma(pa, vt[c2], zero4);
      PIN(dl);
#pragma unroll
      for (int c2 = 0; c2 < 8; ++c2) PIN(d[c2]);
      if (i16 == 4 * kg) { s_ml[kg * 16 + w] = m; s_ml[kg * 16 + 8 + w] = dl[0]; }
#pragma unroll
      for (int c2 = 0; c2 < 8; ++c2) s_ow[(w * 8 + c2) * 64 + l] = d[c2][0];
      CST(5);
      wg_barrier();
      CST(6);
      const f32x4 m0 = *(const f32x4*)(s_ml + kg * 16), m1 = *(const f32x4*)(s_ml + kg * 16 + 4);
      const f32x4 l0 = *(const f32x4*)(s_ml + kg * 16 + 8), l1 = *(const f32x4*)(s_ml + kg * 16 + 12);
      float ov[8];
#pragma unroll
      for (int u = 0; u < 8; ++u) ov[u] = s_ow[(u * 8 + w) * 64 + l];
      const float M = fmx(fmx(fmx(fmx(m0[0], m0[1]), m0[2]), fmx(fmx(m0[3], m1[0]), m1[1])), fmx(m1[2], m1[3]));
      const f32x2 mp[4] = {lo2(m0), hi2(m0), lo2(m1), hi2(m1)}, lp[4] = {lo2(l0), hi2(l0), lo2(l1), hi2(l1)};
      f32x2 L2, o2;
#pragma unroll
      for (int u = 0; u < 4; ++u) {
        const f32x2 e = mp[u] - M;
        const f32x2 c = {__builtin_amdgcn_exp2f(e[0]), __builtin_amdgcn_exp2f(e[1])};
        const f32x2 ou = {ov[2 * u], ov[2 * u + 1]};
        L2 = u ? c * lp[u] + L2 : c * lp[u];
        o2 = u ? c * ou + o2 : c * ou;
      }
      PIN(L2); PIN(o2);
      const float L = L2[0] + L2[1];
      const float on = (o2[0] + o2[1]) * __builtin_amdgcn_rcpf(L);
      const float lw = M + __builtin_amdgcn_logf(L);
      __builtin_amdgcn_raw_buffer_store_b32(__builtin_amdgcn_perm(pk_bf16(on, on), asu(lw), 0x0706000cu | ((l & 3) << 8)) | t8, rs, od, 0, AUX_SC1);
      STAMP(3 + layer * 8);
      CST(7);
    }
    {
      if (layer == 0 && WOPOS == 1) { SCHED(); load_wo(); SCHED(); }
      const int oo = OP_OFF + ((layer * 32 + b * 8 + go) * 8 + (l & 7)) * 2048 + w * 256 + (((l >> 3) & 1) * 4 + kg) * 16;
      const int off[2] = {oo, oo + 128};
      int wd = WP_OFF + (layer * 4 + b) * 16384 + g * 2048 + s * 256 + (l & 1) * 128 + (i16 >> 1) * 16 + (l >> 5) * 8 + (kg & 1) * 4;
      PIN(wd);
      u32x4 v[2];
      DLY(1 + layer * 4);
      if (layer == 0) gather<2, false>(rs, off, t8, v, [&]() {
        if (W1POS == 0) { load_w1(); SCHED(); }
        tr8<4>(wor, wot);
#pragma unroll
        for (int c = 0; c < 8; ++c) PIN(wot[c]);
      }); else gather<2, false>(rs, off, t8, v, [&]() {
        if (__builtin_expect(!__all(((xres ^ t16) & 0xffffu) == 0), 0)) {
          for (u32 spins = 0; spins < SPIN_CAP; ++spins) {
            asm volatile("" ::: "memory");
            xres = __builtin_amdgcn_raw_buffer_load_b32(rs, xo, 0, AUX_SC1);
            if (__all(((xres ^ t16) & 0xffffu) == 0)) break;
          }
        }
        xres &= 0xffff0000u;
        PIN(xres);
      });
      STAMP(4 + layer * 8);
      CST(8);
      const float lw = asf(__builtin_amdgcn_perm(__builtin_amdgcn_perm(v[0][3], v[0][2], 0x05010000u), __builtin_amdgcn_perm(v[0][1], v[0][0], 0x00000501u), 0x07060100u));
      float M = fmx(lw, dpp<QX1>(lw)); M = fmx(M, dpp<QX2>(M)); M = fmx(M, dpp<HMIR>(M));
      float cw = __builtin_amdgcn_exp2f(lw - M);
      float T = cw + dpp<QX1>(cw); T += dpp<QX2>(T); T += dpp<HMIR>(T);
      cw *= __builtin_amdgcn_rcpf(T);
      u32x4 a;
#pragma unroll
      for (int j = 0; j < 4; ++j)
        a[j] = pk_bf16(asf(v[j >> 1][(j & 1) * 2] & 0xffff0000u) * cw, asf(v[j >> 1][(j & 1) * 2 + 1] & 0xffff0000u) * cw);
      f32x4 d[8];
#pragma unroll
      for (int c = 0; c < 8; ++c) d[c] = mfma(a, wot[c], zero4);
      f32x4 z[2];
#pragma unroll
      for (int c = 0; c < 8; ++c) { f32x2 t = lo2(d[c]) + hi2(d[c]); PIN(t); float r = t[0] + t[1]; PIN(r); z[c >> 2][c & 3] = r; }
      if (((l >> 3) & 1) == (l >> 5)) {
        *(f32x4*)(s_wp + (w * 4 + kg) * 64 + (l & 7) * 8) = z[0];
        *(f32x4*)(s_wp + (w * 4 + kg) * 64 + (l & 7) * 8 + 4) = z[1];
      }
      CST(9);
      wg_barrier();
      CST(10);
      if (w == 0) {
        f32x4 p[8];
#pragma unroll
        for (int k = 0; k < 8; ++k) p[k] = *(const f32x4*)(s_wp + (kg * 8 + k) * 64 + i16 * 4);
        const f32x4 acc = ((p[0] + p[1]) + (p[2] + p[3])) + ((p[4] + p[5]) + (p[6] + p[7]));
        const float t = rs16(rs32(acc[0], acc[2]), rs32(acc[1], acc[3]));
        __builtin_amdgcn_raw_buffer_store_b32(tag8(t, t8v), rs, wd, 0, AUX_SC1);
      }
      STAMP(5 + layer * 8);
      CST(11);
    }
    {
      if (layer == 0 && W1POS == 1) { SCHED(); load_w1(); SCHED(); }
      const int wo_ = WP_OFF + (layer * 4 + b) * 16384 + (l >> 3) * 2048 + w * 256 + (l & 7) * 16;
      const int off[2] = {wo_, wo_ + 128};
      int md = MP_OFF + (((layer * 4 + b) * 8 + w) * 56 + g * 7 + s) * 256 + l * 16;
      PIN(md);
      u32x4 v[2];
      DLY(2 + layer * 4);
      if (layer == 0) gather<2>(rs, off, t8, v, [&]() {
        load_w2();
        SCHED();
        tr8<2>(w1r, w1t);
#pragma unroll
        for (int c = 0; c < 4; ++c) PIN(w1t[c]);
      }); else gather<2>(rs, off, t8, v, none);
      STAMP(6 + layer * 8);
      CST(12);
      float y[8];
#pragma unroll
      for (int j = 0; j < 8; ++j) y[j] = asf(v[j >> 2][j & 3]);
      const float x1 = rbf(red8(y)) + asf(xres);
      s_x1[w * 64 + c8] = (uint16_t)(pk_bf16(x1, x1) & 0xffffu);
      asm volatile("" ::: "memory");
      const int xf = w * 64 + (i16 & 1) * 32 + kg * 8;
      const u32x4 am = *(const u32x4*)(s_x1 + (bit3 ? 512 : xf));
      const u32x4 bx = *(const u32x4*)(s_x1 + xf);
      f32x4 d[4];
#pragma unroll
      for (int c = 0; c < 4; ++c) d[c] = mfma(am, w1t[c], zero4);
      f32x4 gr = mfma(am, bx, zero4);
#pragma unroll
      for (int c = 0; c < 4; ++c) PIN(d[c]);
      if (l < 16 || (layer == 0 && kg == 2)) {
        const f32x4 hv = bit3 ? f32x4{d[0][1], d[1][1], d[2][1], d[3][1]} : f32x4{d[0][0], d[1][0], d[2][0], d[3][0]};
        *(f32x4*)(s_hp + (l >> 5) * 512 + (w * 2 + ((l >> 3) & 1)) * 32 + (l & 7) * 4) = hv;
      }
      if ((l & 30) == 0) s_st[(l >> 5) * 16 + w * 2 + (l & 1)] = (l & 1) ? gr[1] : gr[0];
      if (layer == 0) tr8<2>(w2r, w2t);
      wg_barrier();
      if (mlp_on) {
        float st = s_st[l & 31];
        float hp8[8];
#pragma unroll
        for (int p = 0; p < 8; ++p) hp8[p] = s_hp[((l >> 5) * 8 + p) * 32 + (l & 31)];
        if (layer == 0) {
          float ac = 0.f;
#pragma unroll
          for (int p = 0; p < 8; ++p) ac += s_hp[512 + ((l >> 5) * 8 + p) * 32 + (l & 31)];
          cs1 = rs32(ac, ac);
        }
        st += dpp<ROR8>(st); st += dpp<ROR4>(st); st += dpp<ROR2>(st); st += dpp<ROR1>(st);
        float acc = sum8(hp8);
        acc = rs32(acc, acc);
        auto r = __builtin_amdgcn_permlane16_swap(asu(st), asu(st), false, false);
        const float mu = asf(r[1]) * (1.f / 512.f);
        const float var = fmx(asf(r[0]) * (1.f / 512.f) - mu * mu, 0.f);
        const float r12 = __builtin_amdgcn_rsqf(var + EPS * (var + EPS));
        const float hp = (acc - mu * cs1) * r12 + b1v;
        const float u = hp * (-2.302208198144325f - 0.1029432395800235f * (hp * hp));
        const float hv = hp * __builtin_amdgcn_rcpf(1.f + __builtin_amdgcn_exp2f(u));
        s_a[w * 64 + l] = (uint16_t)(pk_bf16(hv, hv) & 0xffffu);
        asm volatile("" ::: "memory");
        const u32x4 ha = *(const u32x4*)(s_a + w * 64 + kg * 8);
        f32x4 e[4];
#pragma unroll
        for (int c = 0; c < 4; ++c) e[c] = mfma(ha, w2t[c], zero4);
#pragma unroll
        for (int c = 0; c < 4; ++c) PIN(e[c]);
        const u32x4 o = {tag8(e[0][0], t8v), tag8(e[1][0], t8v), tag8(e[2][0], t8v), tag8(e[3][0], t8v)};
        if (l < 16) stsc1(rs, md, o);
      }
      STAMP(7 + layer * 8);
      CST(13);
    }
    if (layer == 0) {
      float xr = asf((u32)s_x1[s * 64 + l] << 16);
      PIN(xr);
      const int mo = MP_OFF + ((b * 8 + s) * 56 + w * 7 + kg) * 256 + i16 * 16;
      const int off[2] = {mo, kg < 3 ? mo + 1024 : mo};
      float two = kg < 3 ? 1.f : 0.f;
      int qd = QP_OFF + ((32 + b * 8 + gq) * 8 + s) * 2048 + w * 256 + i16 * 16;
      int sd = ST_OFF + ((b * 8 + gq) * 8 + s) * 128 + (l & 7) * 16 + (l >> 5) * 8;
      int xpi = w * 64 + i16 * 4 + (l >> 5) * 2 + (kg & 1), sai = w * 64 + (i16 & 1) * 32 + kg * 8;
      float b2p = b2a;
      PIN(two); PIN(qd); PIN(sd); PIN(xpi); PIN(sai); PIN(b2p);
      u32x4 v[2];
      DLY(3);
      gather<2>(rs, off, t8, v, none);
      STAMP(8);
      float y[8];
#pragma unroll
      for (int j = 0; j < 4; ++j) y[j] = asf(v[0][j]) + two * asf(v[1][j]);
      s_xp[xpi] = rs16(rs32(y[0], y[2]), rs32(y[1], y[3]));
      wg_barrier();
#pragma unroll
      for (int u = 0; u < 8; ++u) y[u] = s_xp[u * 64 + l];
      const float x2f = rbf(rbf(sum8(y)) + b2p) + xr;
      const u32 x2 = pk_bf16(x2f, x2f);
      s_a[w * 64 + l] = (uint16_t)(x2 & 0xffffu);
      asm volatile("" ::: "memory");
      const u32x4 a = *(const u32x4*)(s_a + sai);
      if (w == 6) {
        const float xv = asf(x2 & 0xffff0000u);
        float u = rs32(xv, xv * xv);
        u = rs16(u, u);
        u += dpp<ROR8>(u); u += dpp<ROR4>(u); u += dpp<ROR2>(u); u += dpp<ROR1>(u);
        const float hi = asf(asu(u) & 0xffffff00u);
        const u32x2 o = {asu(hi) | t8, tag8(u - hi, t8v)};
        if ((l & 24) == 0) __builtin_amdgcn_raw_buffer_store_b64(o, rs, sd, 0, AUX_SC1);
      }
      s1_core(a, qd, l < 16);
      if (w == 7) __builtin_amdgcn_raw_buffer_store_b32((x2 & 0xffff0000u) | t16, rs, X2_OFF + (b * 8 + gq) * 2048 + s * 256 + l * 4, 0, AUX_SC1);
      STAMP(9);
    } else if (w == 0) {
      const int oc = s * 64 + g * 8 + e8;
      const float xr = asf((u32)s_x1[oc] << 16);
      const int mo = MP_OFF + (((4 + b) * 8 + s) * 56 + (l < 56 ? l : 0)) * 256 + g * 32;
      const int off[2] = {mo, mo + 16};
      u32x4 v[2];
      DLY(7);
      gather<2>(rs, off, t8, v, none);
      STAMP(16);
      float y[8];
#pragma unroll
      for (int j = 0; j < 8; ++j) y[j] = l < 56 ? asf(v[j >> 2][j & 3]) : 0.f;
      float t = red8(y);
      t += dpp<QX1>(t); t += dpp<QX2>(t); t += dpp<HMIR>(t);
      const float r = rbf(rbf(t) + b2f) + xr;
      if ((l & 7) == 0) out[b * 512 + oc] = (uint16_t)(pk_bf16(r, r) & 0xffffu);
      if (l == 0 && g == 0 && s == 0) __builtin_amdgcn_raw_buffer_store_b32(tn, rs, EP_OFF + b * 256, 0, AUX_SC1);
      STAMP(17);
    }
  }
}

extern "C" void kernel_launch(void* const* d_in, const int* in_sizes, int n_in, void* d_out, int out_size, void* d_ws,
                              size_t ws_size, hipStream_t stream) {
  (void)in_sizes; (void)n_in; (void)out_size; (void)ws_size;
  fused_decode<<<256, 512, 0, stream>>>((const uint16_t*)d_in[0], (const uint16_t*)d_in[1], (const uint16_t*)d_in[2],
                                        (const uint16_t*)d_in[3], (const uint16_t*)d_in[4], (const uint16_t*)d_in[6],
                                        (const uint16_t*)d_in[7], (const uint16_t*)d_in[8], (const uint16_t*)d_in[9],
                                        (uint16_t*)d_out, (char*)d_ws TUNE_ARG);
}
```

```cpp
#include <hip/hip_runtime.h>
#include <hip/hip_bf16.h>
#include <stdint.h>

#define DIAG 0

typedef unsigned int u32;
typedef __attribute__((ext_vector_type(4))) unsigned int u32x4;
typedef __attribute__((ext_vector_type(2))) unsigned int u32x2;
typedef __attribute__((ext_vector_type(4))) float f32x4;
typedef __attribute__((ext_vector_type(2))) float f32x2;
typedef __attribute__((ext_vector_type(8))) __bf16 bf16x8;
typedef __attribute__((ext_vector_type(2))) __bf16 bf16x2;

namespace {
constexpr int SK = 8192, WIN = 2048, FF = 1792;
constexpr float EPS = 1e-5f;
constexpr u32 SPIN_CAP = 1u << 17;
constexpr int EP_OFF = 0;
constexpr int QP_OFF = 4096;
constexpr int CS_OFF = QP_OFF + 2 * 256 * 2048;
constexpr int X2_OFF = CS_OFF + 256 * 2048;
constexpr int OP_OFF = X2_OFF + 32 * 2048;
constexpr int ST_OFF = OP_OFF + 2 * 256 * 2048;
constexpr int WP_OFF = ST_OFF + 256 * 128;
constexpr int MP_OFF = WP_OFF + 2 * 4 * 16384;
constexpr int WS_END = MP_OFF + 2 * 4 * 8 * 56 * 256;
constexpr int DG_OFF = WS_END;
constexpr int AUX_SC1 = 16;

__device__ __forceinline__ float asf(u32 x) { return __builtin_bit_cast(float, x); }
__device__ __forceinline__ u32 asu(float x) { return __builtin_bit_cast(u32, x); }
__device__ __forceinline__ u32 pk_bf16(float lo, float hi) {
  f32x2 v = {lo, hi};
  return __builtin_bit_cast(u32, __builtin_convertvector(v, bf16x2));
}
__device__ __forceinline__ float rbf(float x) {
  return asf(pk_bf16(x, x) & 0xffff0000u);
}
__device__ __forceinline__ float fmx(float a, float b) { return __builtin_elementwise_maximum(a, b); }
#define PIN(x) asm volatile("" : "+v"(x))
#define SCHED() __builtin_amdgcn_sched_barrier(0)
template <int CTRL> __device__ __forceinline__ float dpp(float x) {
  return asf(__builtin_amdgcn_update_dpp(0, asu(x), CTRL, 0xf, 0xf, true));
}
constexpr int ROR8 = 0x128, ROR4 = 0x124, ROR2 = 0x122, ROR1 = 0x121, QX1 = 0xB1, QX2 = 0x4E, HMIR = 0x141;
__device__ __forceinline__ float rs32(float a, float b) {
  auto r = __builtin_amdgcn_permlane32_swap(asu(a), asu(b), false, false);
  return asf(r[0]) + asf(r[1]);
}
__device__ __forceinline__ float rs16(float a, float b) {
  auto r = __builtin_amdgcn_permlane16_swap(asu(a), asu(b), false, false);
  return asf(r[0]) + asf(r[1]);
}
__device__ __forceinline__ float rs8(float a, float b, bool bit3) {
  return (bit3 ? b : a) + dpp<ROR8>(bit3 ? a : b);
}
__device__ __forceinline__ float mx32(float a) {
  auto r = __builtin_amdgcn_permlane32_swap(asu(a), asu(a), false, false);
  return fmx(asf(r[0]), asf(r[1]));
}
__device__ __forceinline__ float mx16(float a) {
  auto r = __builtin_amdgcn_permlane16_swap(asu(a), asu(a), false, false);
  return fmx(asf(r[0]), asf(r[1]));
}
__device__ __forceinline__ float wave_sum(float a) {
  a += dpp<ROR8>(a); a += dpp<ROR4>(a); a += dpp<ROR2>(a); a += dpp<ROR1>(a);
  a = rs16(a, a); return rs32(a, a);
}
__device__ __forceinline__ f32x4 mfma(u32x4 a, u32x4 b, f32x4 c) {
  return __builtin_amdgcn_mfma_f32_16x16x32_bf16(__builtin_bit_cast(bf16x8, a), __builtin_bit_cast(bf16x8, b), c, 0, 0, 0);
}
template <int NW, typename V> __device__ __forceinline__ void tr8(const V (&r)[8], u32x4 (&t)[2 * NW]) {
#pragma unroll
  for (int c = 0; c < NW; ++c)
#pragma unroll
    for (int j = 0; j < 4; ++j) {
      t[2 * c][j] = __builtin_amdgcn_perm(r[2 * j + 1][c], r[2 * j][c], 0x05040100u);
      t[2 * c + 1][j] = __builtin_amdgcn_perm(r[2 * j + 1][c], r[2 * j][c], 0x07060302u);
    }
}
__device__ __forceinline__ u32x4 ldsc1(__amdgpu_buffer_rsrc_t rs, int off) {
  return __builtin_amdgcn_raw_buffer_load_b128(rs, off, 0, AUX_SC1);
}
__device__ __forceinline__ void stsc1(__amdgpu_buffer_rsrc_t rs, int off, u32x4 v) {
  __builtin_amdgcn_raw_buffer_store_b128(v, rs, off, 0, AUX_SC1);
}
__device__ __forceinline__ u32 tag8(float v, u32 t8) { return (asu(v) & 0xffffff00u) | t8; }
__device__ __forceinline__ u32 or4(u32x4 v) { return (v[0] | v[1]) | (v[2] | v[3]); }
__device__ __forceinline__ void wg_barrier() {
  asm volatile("s_waitcnt lgkmcnt(0)\n\ts_barrier" ::: "memory");
}
template <int N, bool CLEAN> __device__ __forceinline__ bool untag(u32x4 (&v)[N], u32 tg) {
  u32 bad = 0;
#pragma unroll
  for (int k = 0; k < N; ++k) {
    if (CLEAN) { v[k] ^= tg; bad |= or4(v[k]); }
    else bad |= __builtin_amdgcn_bitop3_b32(v[k][0], v[k][1], tg, 0x7e) | __builtin_amdgcn_bitop3_b32(v[k][2], v[k][3], tg, 0x7e);
  }
  return __all((bad & 0xffu) == 0);
}
template <int N, bool CLEAN = true, typename F>
__device__ __forceinline__ void gather(__amdgpu_buffer_rsrc_t rs, const int (&off)[N], u32 tg, u32x4 (&v)[N], F&& behind) {
#pragma unroll
  for (int k = 0; k < N; ++k) v[k] = ldsc1(rs, off[k]);
  SCHED();
  behind();
  SCHED();
  if (__builtin_expect(!untag<N, CLEAN>(v, tg), 0)) {
    for (u32 spins = 0; spins < SPIN_CAP; ++spins) {
      asm volatile("" ::: "memory");
#pragma unroll
      for (int k = 0; k < N; ++k) v[k] = ldsc1(rs, off[k]);
      if (untag<N, CLEAN>(v, tg)) break;
    }
  }
}
__device__ __forceinline__ f32x2 lo2(f32x4 v) { return __builtin_shufflevector(v, v, 0, 1); }
__device__ __forceinline__ f32x2 hi2(f32x4 v) { return __builtin_shufflevector(v, v, 2, 3); }
__device__ __forceinline__ float sum8(const float (&y)[8]) {
  f32x2 t = (f32x2{y[0], y[1]} + f32x2{y[2], y[3]}) + (f32x2{y[4], y[5]} + f32x2{y[6], y[7]});
  PIN(t);
  return t[0] + t[1];
}
}

#define STAMP(i) do {} while (0)
#define CST(i) do {} while (0)
#define KDELAY {0, 18, 18, 18, 18, 18, 18, 18}
constexpr int kDelay[8] = KDELAY;
#define DLY(i) do { if (kDelay[i] > 0) asm volatile("s_sleep %0" :: "i"(kDelay[i])); } while (0)
#define TUNE_PARAM
#define TUNE_ARG
#define QSHIFT 1
#define LORD 3
#define LBAR 7
#define TEST 0
#define WOPOS 0
#define W1POS 1
#define WARM 0
#define OSHIFT 1

__global__ void __launch_bounds__(512, 2)
fused_decode(const uint16_t* __restrict__ x, const uint16_t* __restrict__ kc, const uint16_t* __restrict__ vc,
             const uint16_t* __restrict__ wq, const uint16_t* __restrict__ wo, const uint16_t* __restrict__ w1,
             const uint16_t* __restrict__ b1, const uint16_t* __restrict__ w2, const uint16_t* __restrict__ b2,
             uint16_t* __restrict__ out, char* ws TUNE_PARAM) {
  __shared__ __attribute__((aligned(16))) float s_ow[64 * 64];
  __shared__ __attribute__((aligned(16))) float s_ml[4 * 16];
  __shared__ __attribute__((aligned(16))) uint16_t s_q[512];
  __shared__ __attribute__((aligned(16))) uint16_t s_x1[512 + 8];
  __shared__ __attribute__((aligned(16))) float s_hp[2 * 16 * 32];
  __shared__ __attribute__((aligned(16))) float s_st[32];
  __shared__ __attribute__((aligned(16))) uint16_t s_a[8 * 64];
  __shared__ __attribute__((aligned(16))) float s_xp[8 * 64];
  __shared__ __attribute__((aligned(16))) float s_wp[32 * 64];

  const int tid = threadIdx.x, bid = blockIdx.x;
  const int w = __builtin_amdgcn_readfirstlane(tid >> 6);
  const int l = tid & 63, i16 = l & 15, kg = l >> 4;
  const int g = bid & 7, s = (bid >> 3) & 7, b = bid >> 6;
  const int bg = b * 8 + g;
  const bool bit3 = (l & 8) != 0;
  const bool mlp_on = s < 7;
  const int f0 = g * 224 + (mlp_on ? s : 6) * 32;
  const __amdgpu_buffer_rsrc_t rs = __builtin_amdgcn_make_buffer_rsrc((void*)ws, 0, WS_END, 0x00020000);
  STAMP(0);
  const int gq = (g + QSHIFT) & 7, go = (g + OSHIFT) & 7;

  const u32 ep = __builtin_amdgcn_raw_buffer_load_b32(rs, EP_OFF + b * 256, 0, AUX_SC1);
  const u32x4 xfull = *(const u32x4*)(x + b * 512 + l * 8);
  const u32x4 xa0 = *(const u32x4*)(x + b * 512 + s * 64 + (i16 & 1) * 32 + kg * 8);
  const int e8 = (l >> 5) * 4 + ((l >> 4) & 1) * 2 + ((l >> 3) & 1);
  const int c8 = (l & 7) * 8 + e8;
  u32x4 wqr[8], kr[8], vr[8], wor[8];
  u32x2 w1r[8], w2r[8];
  {
    const uint16_t* p = wq + (size_t)(s * 64 + ((l >> 3) & 1) * 32 + kg * 8) * 4096 + gq * 512 + w * 64 + (l & 7) * 8;
#pragma unroll
    for (int r = 0; r < 8; ++r) wqr[r] = *(const u32x4*)(p + r * 4096);
  }
  u32 warm = 0;
  __builtin_amdgcn_sched_barrier(0);
  asm volatile("s_barrier" ::: "memory");
  __builtin_amdgcn_sched_barrier(0);
  const size_t kvbase = ((size_t)bg * SK + (SK - WIN) + s * 256 + w * 32) * 128;
  {
    const uint16_t* p = kc + kvbase + (i16 >> 2) * 8 * 128 + kg * 32 + (i16 & 3) * 8;
#pragma unroll
    for (int m = 0; m < 8; ++m) kr[m] = __builtin_nontemporal_load((const u32x4*)(p + m * 128));
  }
  auto load_v = [&](int r0, int r1) {
    const uint16_t* p = vc + kvbase + kg * 8 * 128 + i16 * 8;
#pragma unroll
    for (int r = r0; r < r1; ++r) vr[r] = __builtin_nontemporal_load((const u32x4*)(p + r * 128));
  };
  if (LORD == 0) load_v(0, 8);
  __builtin_amdgcn_sched_barrier(0);
  if (LBAR & 1) asm volatile("s_barrier" ::: "memory");
  const float b1v = asf((u32)b1[f0 + (l & 31)] << 16);
  const float b2a = asf((u32)b2[s * 64 + l] << 16);
  const float b2f = asf((u32)b2[s * 64 + g * 8 + e8] << 16);
  u32 xres = (u32)x[b * 512 + w * 64 + c8] << 16;
  __builtin_amdgcn_sched_barrier(0);
  const u32 tn = __builtin_amdgcn_readfirstlane(ep) + 1u;
  const u32 t8 = tn & 0xffu, t16 = tn & 0xffffu;
  u32 t8v = t8;
  PIN(t8v);

  u32x4 ya0;
  {
    float xv[8];
#pragma unroll
    for (int j = 0; j < 4; ++j) { xv[2 * j] = asf(xfull[j] << 16); xv[2 * j + 1] = asf(xfull[j] & 0xffff0000u); }
    float sm = 0.f;
#pragma unroll
    for (int j = 0; j < 8; ++j) sm += xv[j];
    const float mean = wave_sum(sm) * (1.f / 512.f);
    float sq = 0.f;
#pragma unroll
    for (int j = 0; j < 8; ++j) sq += (xv[j] - mean) * (xv[j] - mean);
    const float mb = rbf(mean);
    const float rsd = __builtin_amdgcn_rcpf(rbf(__builtin_amdgcn_sqrtf(rbf(rbf(wave_sum(sq) * (1.f / 512.f)) + EPS))));
#pragma unroll
    for (int j = 0; j < 4; ++j) {
      const u32 c = pk_bf16(asf(xa0[j] << 16) - mb, asf(xa0[j] & 0xffff0000u) - mb);
      ya0[j] = pk_bf16(asf(c << 16) * rsd, asf(c & 0xffff0000u) * rsd);
    }
  }
  u32x4 wqt[8];
  tr8<4>(wqr, wqt);
  u32x4 ones = {0x3f803f80u, 0x3f803f80u, 0x3f803f80u, 0x3f803f80u};
  PIN(ones);
  auto none = []() {};
  const f32x4 zero4 = {0.f, 0.f, 0.f, 0.f};

  auto s1_core = [&](u32x4 a, int dst, bool on) {
    f32x4 d[8];
#pragma unroll
    for (int c = 0; c < 8; ++c) d[c] = mfma(a, wqt[c], zero4);
#pragma unroll
    for (int c = 0; c < 8; ++c) PIN(d[c]);
    float keep[4], send[4];
#pragma unroll
    for (int j = 0; j < 4; ++j) send[j] = bit3 ? d[j][1] : d[4 + j][0];
#pragma unroll
    for (int j = 0; j < 4; ++j) keep[j] = bit3 ? d[4 + j][1] : d[j][0];
    SCHED();
    u32x4 o;
#pragma unroll
    for (int j = 0; j < 4; ++j) { float t = keep[j] + dpp<ROR8>(send[j]); PIN(t); o[j] = tag8(t, t8v); }
    if (on) stsc1(rs, dst, o);
  };
  if (tid < 4) ((u32*)(s_x1 + 512))[tid] = 0x3f803f80u;
  s1_core(bit3 ? ones : ya0, (kg == 0 ? QP_OFF : CS_OFF) + ((b * 8 + gq) * 8 + s) * 2048 + w * 256 + i16 * 16, (kg & 1) == 0);
  STAMP(1);
  __builtin_amdgcn_sched_barrier(0);
  if (LBAR & 2) asm volatile("s_barrier" ::: "memory");
  __builtin_amdgcn_sched_barrier(0);
  if (LORD == 2) load_v(0, 4);
  if (LORD == 3) load_v(0, 8);
  __builtin_amdgcn_sched_barrier(0);
  if (LBAR & 4) asm volatile("s_barrier" ::: "memory");
  __builtin_amdgcn_sched_barrier(0);
  auto load_wo = [&]() {
    const uint16_t* p = wo + (size_t)(go * 512 + (((l >> 3) & 1) * 4 + kg) * 64 + w) * 512 + s * 64 + (l & 7) * 8;
#pragma unroll
    for (int r = 0; r < 8; ++r) wor[r] = *(const u32x4*)(p + r * 8 * 512);
  };
  auto load_w1 = [&]() {
    const uint16_t* p = w1 + (size_t)(w * 64 + ((l >> 3) & 1) * 32 + kg * 8) * FF + f0 + (l & 7) * 4;
#pragma unroll
    for (int r = 0; r < 8; ++r) w1r[r] = *(const u32x2*)(p + r * FF);
  };
  auto load_w2 = [&]() {
    const uint16_t* p2 = w2 + (size_t)(f0 + kg * 8) * 512 + w * 64 + i16 * 4;
#pragma unroll
    for (int r = 0; r < 8; ++r) w2r[r] = *(const u32x2*)(p2 + r * 512);
  };

  asm volatile("" :: "v"(warm));
  const int xo = X2_OFF + bg * 2048 + w * 256 + c8 * 4;
  u32x4 vt[8], wot[8], w1t[4], w2t[4];
  float csq = 0.f, cs1 = 0.f;
  auto red8 = [&](const float (&v)[8]) {
    const float u0 = rs32(v[0], v[4]), u1 = rs32(v[1], v[5]), u2 = rs32(v[2], v[6]), u3 = rs32(v[3], v[7]);
    return rs8(rs16(u0, u2), rs16(u1, u3), bit3);
  };

#pragma unroll
  for (int layer = 0; layer < 2; ++layer) {
    {
      const int qo = (bg * 8 + (l >> 3)) * 2048 + w * 256 + (l & 7) * 16;
      int od = OP_OFF + ((layer * 32 + bg) * 8 + s) * 2048 + w * 256 + ((l >> 2) & 1) * 128 + (l >> 3) * 16 + (l & 3) * 4;
      PIN(od);
      float a[8], mean = 0.f, rstd = 1.f;
      DLY(layer * 4);
      if (layer == 0) {
        u32x4 v[4];
        const int off[4] = {QP_OFF + qo, QP_OFF + qo + 128, CS_OFF + qo, CS_OFF + qo + 128};
        gather<4>(rs, off, t8, v, [&]() { if (WOPOS == 0) load_wo(); });
        STAMP(2);
        float c[8];
#pragma unroll
        for (int j = 0; j < 4; ++j) { a[j] = asf(v[0][j]); a[4 + j] = asf(v[1][j]); c[j] = asf(v[2][j]); c[4 + j] = asf(v[3][j]); }
        csq = red8(c);
      } else {
        u32x4 v[3];
        const int off[3] = {ST_OFF + (bg * 8 + (l >> 3)) * 128 + (l & 7) * 16, QP_OFF + 256 * 2048 + qo, QP_OFF + 256 * 2048 + qo + 128};
        gather<3>(rs, off, t8, v, [&]() { xres = __builtin_amdgcn_raw_buffer_load_b32(rs, xo, 0, AUX_SC1); });
        STAMP(10);
        CST(0);
#pragma unroll
        for (int j = 0; j < 4; ++j) { a[j] = asf(v[1][j]); a[4 + j] = asf(v[2][j]); }
        float u = rs32(asf(v[0][0]) + asf(v[0][1]), asf(v[0][2]) + asf(v[0][3]));
        u = rs16(u, u);
        u += dpp<ROR8>(u);
        auto r = __builtin_amdgcn_permlane32_swap(asu(u), asu(u), false, false);
        mean = asf(r[0]) * (1.f / 512.f);
        rstd = __builtin_amdgcn_rsqf(fmx(asf(r[1]) * (1.f / 512.f) - mean * mean, 0.f) + EPS);
      }
      const float qv = layer == 0 ? red8(a) : (red8(a) - mean * csq) * rstd;
      s_q[w * 64 + c8] = (uint16_t)(pk_bf16(qv, qv) & 0xffffu);
      CST(1);
      wg_barrier();
      CST(2);
      const u32x4 qf = *(const u32x4*)(s_q + (i16 >> 2) * 128 + kg * 32 + (i16 & 3) * 8);
      f32x4 d[8];
#pragma unroll
      for (int m = 0; m < 8; ++m) d[m] = mfma(kr[m], qf, zero4);
      SCHED();
      float sv[8];
#pragma unroll
      for (int m = 0; m < 8; ++m) { sv[m] = d[m][0] + dpp<0x55>(d[m][1]); PIN(sv[m]); }
#pragma unroll
      for (int m = 0; m < 8; ++m) { sv[m] += dpp<0xAA>(d[m][2]); PIN(sv[m]); }
#pragma unroll
      for (int m = 0; m < 8; ++m) { sv[m] += dpp<0xFF>(d[m][3]); PIN(sv[m]); }
      if (layer == 0) tr8<4>(vr, vt);
      CST(3);
      if (layer == 0) {
#pragma unroll
        for (int j = 0; j < 4; ++j) {
          const u32 c = pk_bf16(sv[2 * j], sv[2 * j + 1]);
          const f32x2 t = f32x2{asf(c << 16), asf(c & 0xffff0000u)} * 0.08838834764831845f;
          const u32 e = pk_bf16(t[0], t[1]);
          sv[2 * j] = asf(e << 16); sv[2 * j + 1] = asf(e & 0xffff0000u);
        }
      }
      const float sc = (layer == 0 ? 1.f : 0.08838834764831845f) * 1.4426950408889634f;
      float m = fmx(fmx(fmx(fmx(sv[0], sv[1]), sv[2]), fmx(fmx(sv[3], sv[4]), sv[5])), fmx(sv[6], sv[7]));
      m = mx32(mx16(m)) * sc;
      u32x4 pa;
#pragma unroll
      for (int j = 0; j < 4; ++j) {
        const f32x2 e = f32x2{sv[2 * j], sv[2 * j + 1]} * sc - m;
        pa[j] = pk_bf16(__builtin_amdgcn_exp2f(e[0]), __builtin_amdgcn_exp2f(e[1]));
      }
      CST(4);
      f32x4 dl = mfma(pa, ones, zero4);
      SCHED();
#pragma unroll
      for (int c2 = 0; c2 < 8; ++c2) d[c2] = mfma(pa, vt[c2], zero4);
      PIN(dl);
#pragma unroll
      for (int c2 = 0; c2 < 8; ++c2) PIN(d[c2]);
      if (i16 == 4 * kg) { s_ml[kg * 16 + w] = m; s_ml[kg * 16 + 8 + w] = dl[0]; }
#pragma unroll
      for (int c2 = 0; c2 < 8; ++c2) s_ow[(w * 8 + c2) * 64 + l] = d[c2][0];
      CST(5);
      wg_barrier();
      CST(6);
      const f32x4 m0 = *(const f32x4*)(s_ml + kg * 16), m1 = *(const f32x4*)(s_ml + kg * 16 + 4);
      const f32x4 l0 = *(const f32x4*)(s_ml + kg * 16 + 8), l1 = *(const f32x4*)(s_ml + kg * 16 + 12);
      float ov[8];
#pragma unroll
      for (int u = 0; u < 8; ++u) ov[u] = s_ow[(u * 8 + w) * 64 + l];
      const float M = fmx(fmx(fmx(fmx(m0[0], m0[1]), m0[2]), fmx(fmx(m0[3], m1[0]), m1[1])), fmx(m1[2], m1[3]));
      const f32x2 mp[4] = {lo2(m0), hi2(m0), lo2(m1), hi2(m1)}, lp[4] = {lo2(l0), hi2(l0), lo2(l1), hi2(l1)};
      f32x2 L2, o2;
#pragma unroll
      for (int u = 0; u < 4; ++u) {
        const f32x2 e = mp[u] - M;
        const f32x2 c = {__builtin_amdgcn_exp2f(e[0]), __builtin_amdgcn_exp2f(e[1])};
        const f32x2 ou = {ov[2 * u], ov[2 * u + 1]};
        L2 = u ? c * lp[u] + L2 : c * lp[u];
        o2 = u ? c * ou + o2 : c * ou;
      }
      PIN(L2); PIN(o2);
      const float L = L2[0] + L2[1];
      const float on = (o2[0] + o2[1]) * __builtin_amdgcn_rcpf(L);
      const float lw = M + __builtin_amdgcn_logf(L);
      __builtin_amdgcn_raw_buffer_store_b32(__builtin_amdgcn_perm(pk_bf16(on, on), asu(lw), 0x0706000cu | ((l & 3) << 8)) | t8, rs, od, 0, AUX_SC1);
      STAMP(3 + layer * 8);
      CST(7);
    }
    {
      if (layer == 0 && WOPOS == 1) { SCHED(); load_wo(); SCHED(); }
      const int oo = OP_OFF + ((layer * 32 + b * 8 + go) * 8 + (l & 7)) * 2048 + w * 256 + (((l >> 3) & 1) * 4 + kg) * 16;
      const int off[2] = {oo, oo + 128};
      int wd = WP_OFF + (layer * 4 + b) * 16384 + g * 2048 + s * 256 + (l & 1) * 128 + (i16 >> 1) * 16 + (l >> 5) * 8 + (kg & 1) * 4;
      PIN(wd);
      u32x4 v[2];
      DLY(1 + layer * 4);
      if (layer == 0) gather<2, false>(rs, off, t8, v, [&]() {
        if (W1POS == 0) { load_w1(); SCHED(); }
        tr8<4>(wor, wot);
#pragma unroll
        for (int c = 0; c < 8; ++c) PIN(wot[c]);
      }); else gather<2, false>(rs, off, t8, v, [&]() {
        if (__builtin_expect(!__all(((xres ^ t16) & 0xffffu) == 0), 0)) {
          for (u32 spins = 0; spins < SPIN_CAP; ++spins) {
            asm volatile("" ::: "memory");
            xres = __builtin_amdgcn_raw_buffer_load_b32(rs, xo, 0, AUX_SC1);
            if (__all(((xres ^ t16) & 0xffffu) == 0)) break;
          }
        }
        xres &= 0xffff0000u;
        PIN(xres);
      });
      STAMP(4 + layer * 8);
      CST(8);
      const float lw = asf(__builtin_amdgcn_perm(__builtin_amdgcn_perm(v[0][3], v[0][2], 0x05010000u), __builtin_amdgcn_perm(v[0][1], v[0][0], 0x00000501u), 0x07060100u));
      float M = fmx(lw, dpp<QX1>(lw)); M = fmx(M, dpp<QX2>(M)); M = fmx(M, dpp<HMIR>(M));
      float cw = __builtin_amdgcn_exp2f(lw - M);
      float T = cw + dpp<QX1>(cw); T += dpp<QX2>(T); T += dpp<HMIR>(T);
      cw *= __builtin_amdgcn_rcpf(T);
      u32x4 a;
#pragma unroll
      for (int j = 0; j < 4; ++j)
        a[j] = pk_bf16(asf(v[j >> 1][(j & 1) * 2] & 0xffff0000u) * cw, asf(v[j >> 1][(j & 1) * 2 + 1] & 0xffff0000u) * cw);
      f32x4 d[8];
#pragma unroll
      for (int c = 0; c < 8; ++c) d[c] = mfma(a, wot[c], zero4);
      f32x4 z[2];
#pragma unroll
      for (int c = 0; c < 8; ++c) { f32x2 t = lo2(d[c]) + hi2(d[c]); PIN(t); float r = t[0] + t[1]; PIN(r); z[c >> 2][c & 3] = r; }
      if (((l >> 3) & 1) == (l >> 5)) {
        *(f32x4*)(s_wp + (w * 4 + kg) * 64 + (l & 7) * 8) = z[0];
        *(f32x4*)(s_wp + (w * 4 + kg) * 64 + (l & 7) * 8 + 4) = z[1];
      }
      CST(9);
      wg_barrier();
      CST(10);
      if (w == 0) {
        f32x4 p[8];
#pragma unroll
        for (int k = 0; k < 8; ++k) p[k] = *(const f32x4*)(s_wp + (kg * 8 + k) * 64 + i16 * 4);
        const f32x4 acc = ((p[0] + p[1]) + (p[2] + p[3])) + ((p[4] + p[5]) + (p[6] + p[7]));
        const float t = rs16(rs32(acc[0], acc[2]), rs32(acc[1], acc[3]));
        __builtin_amdgcn_raw_buffer_store_b32(tag8(t, t8v), rs, wd, 0, AUX_SC1);
      }
      STAMP(5 + layer * 8);
      CST(11);
    }
    {
      if (layer == 0 && W1POS == 1) { SCHED(); load_w1(); SCHED(); }
      const int wo_ = WP_OFF + (layer * 4 + b) * 16384 + (l >> 3) * 2048 + w * 256 + (l & 7) * 16;
      const int off[2] = {wo_, wo_ + 128};
      int md = MP_OFF + (((layer * 4 + b) * 8 + w) * 56 + g * 7 + s) * 256 + l * 16;
      PIN(md);
      u32x4 v[2];
      DLY(2 + layer * 4);
      if (layer == 0) gather<2>(rs, off, t8, v, [&]() {
        load_w2();
        SCHED();
        tr8<2>(w1r, w1t);
#pragma unroll
        for (int c = 0; c < 4; ++c) PIN(w1t[c]);
      }); else gather<2>(rs, off, t8, v, none);
      STAMP(6 + layer * 8);
      CST(12);
      float y[8];
#pragma unroll
      for (int j = 0; j < 8; ++j) y[j] = asf(v[j >> 2][j & 3]);
      const float x1 = rbf(red8(y)) + asf(xres);
      s_x1[w * 64 + c8] = (uint16_t)(pk_bf16(x1, x1) & 0xffffu);
      asm volatile("" ::: "memory");
      const int xf = w * 64 + (i16 & 1) * 32 + kg * 8;
      const u32x4 am = *(const u32x4*)(s_x1 + (bit3 ? 512 : xf));
      const u32x4 bx = *(const u32x4*)(s_x1 + xf);
      f32x4 d[4];
#pragma unroll
      for (int c = 0; c < 4; ++c) d[c] = mfma(am, w1t[c], zero4);
      f32x4 gr = mfma(am, bx, zero4);
#pragma unroll
      for (int c = 0; c < 4; ++c) PIN(d[c]);
      if (l < 16 || (layer == 0 && kg == 2)) {
        const f32x4 hv = bit3 ? f32x4{d[0][1], d[1][1], d[2][1], d[3][1]} : f32x4{d[0][0], d[1][0], d[2][0], d[3][0]};
        *(f32x4*)(s_hp + (l >> 5) * 512 + (w * 2 + ((l >> 3) & 1)) * 32 + (l & 7) * 4) = hv;
      }
      if ((l & 30) == 0) s_st[(l >> 5) * 16 + w * 2 + (l & 1)] = (l & 1) ? gr[1] : gr[0];
      if (layer == 0) tr8<2>(w2r, w2t);
      wg_barrier();
      if (mlp_on) {
        float st = s_st[l & 31];
        float hp8[8];
#pragma unroll
        for (int p = 0; p < 8; ++p) hp8[p] = s_hp[((l >> 5) * 8 + p) * 32 + (l & 31)];
        if (layer == 0) {
          float ac = 0.f;
#pragma unroll
          for (int p = 0; p < 8; ++p) ac += s_hp[512 + ((l >> 5) * 8 + p) * 32 + (l & 31)];
          cs1 = rs32(ac, ac);
        }
        st += dpp<ROR8>(st); st += dpp<ROR4>(st); st += dpp<ROR2>(st); st += dpp<ROR1>(st);
        float acc = sum8(hp8);
        acc = rs32(acc, acc);
        auto r = __builtin_amdgcn_permlane16_swap(asu(st), asu(st), false, false);
        const float mu = asf(r[1]) * (1.f / 512.f);
        const float var = fmx(asf(r[0]) * (1.f / 512.f) - mu * mu, 0.f);
        const float r12 = __builtin_amdgcn_rsqf(var + EPS * (var + EPS));
        const float hp = (acc - mu * cs1) * r12 + b1v;
        const float u = hp * (-2.302208198144325f - 0.1029432395800235f * (hp * hp));
        const float hv = hp * __builtin_amdgcn_rcpf(1.f + __builtin_amdgcn_exp2f(u));
        s_a[w * 64 + l] = (uint16_t)(pk_bf16(hv, hv) & 0xffffu);
        asm volatile("" ::: "memory");
        const u32x4 ha = *(const u32x4*)(s_a + w * 64 + kg * 8);
        f32x4 e[4];
#pragma unroll
        for (int c = 0; c < 4; ++c) e[c] = mfma(ha, w2t[c], zero4);
#pragma unroll
        for (int c = 0; c < 4; ++c) PIN(e[c]);
        const u32x4 o = {tag8(e[0][0], t8v), tag8(e[1][0], t8v), tag8(e[2][0], t8v), tag8(e[3][0], t8v)};
        if (l < 16) stsc1(rs, md, o);
      }
      STAMP(7 + layer * 8);
      CST(13);
    }
    if (layer == 0) {
      float xr = asf((u32)s_x1[s * 64 + l] << 16);
      PIN(xr);
      const int mo = MP_OFF + ((b * 8 + s) * 56 + w * 7 + kg) * 256 + i16 * 16;
      const int off[2] = {mo, kg < 3 ? mo + 1024 : mo};
      float two = kg < 3 ? 1.f : 0.f;
      int qd = QP_OFF + ((32 + b * 8 + gq) * 8 + s) * 2048 + w * 256 + i16 * 16;
      int sd = ST_OFF + ((b * 8 + gq) * 8 + s) * 128 + (l & 7) * 16 + (l >> 5) * 8;
      int xpi = w * 64 + i16 * 4 + (l >> 5) * 2 + (kg & 1), sai = w * 64 + (i16 & 1) * 32 + kg * 8;
      float b2p = b2a;
      PIN(two); PIN(qd); PIN(sd); PIN(xpi); PIN(sai); PIN(b2p);
      u32x4 v[2];
      DLY(3);
      gather<2>(rs, off, t8, v, none);
      STAMP(8);
      float y[8];
#pragma unroll
      for (int j = 0; j < 4; ++j) y[j] = asf(v[0][j]) + two * asf(v[1][j]);
      s_xp[xpi] = rs16(rs32(y[0], y[2]), rs32(y[1], y[3]));
      wg_barrier();
#pragma unroll
      for (int u = 0; u < 8; ++u) y[u] = s_xp[u * 64 + l];
      const float x2f = rbf(rbf(sum8(y)) + b2p) + xr;
      const u32 x2 = pk_bf16(x2f, x2f);
      s_a[w * 64 + l] = (uint16_t)(x2 & 0xffffu);
      asm volatile("" ::: "memory");
      const u32x4 a = *(const u32x4*)(s_a + sai);
      if (w == 6) {
        const float xv = asf(x2 & 0xffff0000u);
        float u = rs32(xv, xv * xv);
        u = rs16(u, u);
        u += dpp<ROR8>(u); u += dpp<ROR4>(u); u += dpp<ROR2>(u); u += dpp<ROR1>(u);
        const float hi = asf(asu(u) & 0xffffff00u);
        const u32x2 o = {asu(hi) | t8, tag8(u - hi, t8v)};
        if ((l & 24) == 0) __builtin_amdgcn_raw_buffer_store_b64(o, rs, sd, 0, AUX_SC1);
      }
      s1_core(a, qd, l < 16);
      if (w == 7) __builtin_amdgcn_raw_buffer_store_b32((x2 & 0xffff0000u) | t16, rs, X2_OFF + (b * 8 + gq) * 2048 + s * 256 + l * 4, 0, AUX_SC1);
      STAMP(9);
    } else if (w == 0) {
      const int oc = s * 64 + g * 8 + e8;
      const float xr = asf((u32)s_x1[oc] << 16);
      const int mo = MP_OFF + (((4 + b) * 8 + s) * 56 + (l < 56 ? l : 0)) * 256 + g * 32;
      const int off[2] = {mo, mo + 16};
      u32x4 v[2];
      DLY(7);
      gather<2>(rs, off, t8, v, none);
      STAMP(16);
      float y[8];
#pragma unroll
      for (int j = 0; j < 8; ++j) y[j] = l < 56 ? asf(v[j >> 2][j & 3]) : 0.f;
      float t = red8(y);
      t += dpp<QX1>(t); t += dpp<QX2>(t); t += dpp<HMIR>(t);
      const float r = rbf(rbf(t) + b2f) + xr;
      if ((l & 7) == 0) out[b * 512 + oc] = (uint16_t)(pk_bf16(r, r) & 0xffffu);
      if (l == 0 && g == 0 && s == 0) __builtin_amdgcn_raw_buffer_store_b32(tn, rs, EP_OFF + b * 256, 0, AUX_SC1);
      STAMP(17);
    }
  }
}

extern "C" void kernel_launch(void* const* d_in, const int* in_sizes, int n_in, void* d_out, int out_size, void* d_ws,
                              size_t ws_size, hipStream_t stream) {
  (void)in_sizes; (void)n_in; (void)out_size; (void)ws_size;
  fused_decode<<<256, 512, 0, stream>>>((const uint16_t*)d_in[0], (const uint16_t*)d_in[1], (const uint16_t*)d_in[2],
                                        (const uint16_t*)d_in[3], (const uint16_t*)d_in[4], (const uint16_t*)d_in[6],
                                        (const uint16_t*)d_in[7], (const uint16_t*)d_in[8], (const uint16_t*)d_in[9],
                                        (uint16_t*)d_out, (char*)d_ws TUNE_ARG);
}
```

```cpp
#include <hip/hip_runtime.h>
#include <hip/hip_bf16.h>
#include <stdint.h>

#define DIAG 0

typedef unsigned int u32;
typedef __attribute__((ext_vector_type(4))) unsigned int u32x4;
typedef __attribute__((ext_vector_type(2))) unsigned int u32x2;
typedef __attribute__((ext_vector_type(4))) float f32x4;
typedef __attribute__((ext_vector_type(2))) float f32x2;
typedef __attribute__((ext_vector_type(8))) __bf16 bf16x8;
typedef __attribute__((ext_vector_type(2))) __bf16 bf16x2;

namespace {
constexpr int SK = 8192, WIN = 2048, FF = 1792;
constexpr float EPS = 1e-5f;
constexpr u32 SPIN_CAP = 1u << 17;
constexpr int EP_OFF = 0;
constexpr int QP_OFF = 4096;
constexpr int CS_OFF = QP_OFF + 2 * 256 * 2048;
constexpr int X2_OFF = CS_OFF + 256 * 2048;
constexpr int OP_OFF = X2_OFF + 32 * 2048;
constexpr int ST_OFF = OP_OFF + 2 * 256 * 2048;
constexpr int WP_OFF = ST_OFF + 256 * 128;
constexpr int MP_OFF = WP_OFF + 2 * 4 * 16384;
constexpr int WS_END = MP_OFF + 2 * 4 * 8 * 56 * 256;
constexpr int DG_OFF = WS_END;
constexpr int AUX_SC1 = 16;

__device__ __forceinline__ float asf(u32 x) { return __builtin_bit_cast(float, x); }
__device__ __forceinline__ u32 asu(float x) { return __builtin_bit_cast(u32, x); }
__device__ __forceinline__ u32 pk_bf16(float lo, float hi) {
  f32x2 v = {lo, hi};
  return __builtin_bit_cast(u32, __builtin_convertvector(v, bf16x2));
}
__device__ __forceinline__ float rbf(float x) {
  return asf(pk_bf16(x, x) & 0xffff0000u);
}
__device__ __forceinline__ float fmx(float a, float b) { return __builtin_elementwise_maximum(a, b); }
#define PIN(x) asm volatile("" : "+v"(x))
#define SCHED() __builtin_amdgcn_sched_barrier(0)
template <int CTRL> __device__ __forceinline__ float dpp(float x) {
  return asf(__builtin_amdgcn_update_dpp(0, asu(x), CTRL, 0xf, 0xf, true));
}
constexpr int ROR8 = 0x128, ROR4 = 0x124, ROR2 = 0x122, ROR1 = 0x121, QX1 = 0xB1, QX2 = 0x4E, HMIR = 0x141;
__device__ __forceinline__ float rs32(float a, float b) {
  auto r = __builtin_amdgcn_permlane32_swap(asu(a), asu(b), false, false);
  return asf(r[0]) + asf(r[1]);
}
__device__ __forceinline__ float rs16(float a, float b) {
  auto r = __builtin_amdgcn_permlane16_swap(asu(a), asu(b), false, false);
  return asf(r[0]) + asf(r[1]);
}
__device__ __forceinline__ float rs8(float a, float b, bool bit3) {
  return (bit3 ? b : a) + dpp<ROR8>(bit3 ? a : b);
}
__device__ __forceinline__ float mx32(float a) {
  auto r = __builtin_amdgcn_permlane32_swap(asu(a), asu(a), false, false);
  return fmx(asf(r[0]), asf(r[1]));
}
__device__ __forceinline__ float mx16(float a) {
  auto r = __builtin_amdgcn_permlane16_swap(asu(a), asu(a), false, false);
  return fmx(asf(r[0]), asf(r[1]));
}
__device__ __forceinline__ float wave_sum(float a) {
  a += dpp<ROR8>(a); a += dpp<ROR4>(a); a += dpp<ROR2>(a); a += dpp<ROR1>(a);
  a = rs16(a, a); return rs32(a, a);
}
__device__ __forceinline__ f32x4 mfma(u32x4 a, u32x4 b, f32x4 c) {
  return __builtin_amdgcn_mfma_f32_16x16x32_bf16(__builtin_bit_cast(bf16x8, a), __builtin_bit_cast(bf16x8, b), c, 0, 0, 0);
}
template <int NW, typename V> __device__ __forceinline__ void tr8(const V (&r)[8], u32x4 (&t)[2 * NW]) {
#pragma unroll
  for (int c = 0; c < NW; ++c)
#pragma unroll
    for (int j = 0; j < 4; ++j) {
      t[2 * c][j] = __builtin_amdgcn_perm(r[2 * j + 1][c], r[2 * j][c], 0x05040100u);
      t[2 * c + 1][j] = __builtin_amdgcn_perm(r[2 * j + 1][c], r[2 * j][c], 0x07060302u);
    }
}
__device__ __forceinline__ u32x4 ldsc1(__amdgpu_buffer_rsrc_t rs, int off) {
  return __builtin_amdgcn_raw_buffer_load_b128(rs, off, 0, AUX_SC1);
}
__device__ __forceinline__ void stsc1(__amdgpu_buffer_rsrc_t rs, int off, u32x4 v) {
  __builtin_amdgcn_raw_buffer_store_b128(v, rs, off, 0, AUX_SC1);
}
__device__ __forceinline__ u32 tag8(float v, u32 t8) { return (asu(v) & 0xffffff00u) | t8; }
__device__ __forceinline__ u32 or4(u32x4 v) { return (v[0] | v[1]) | (v[2] | v[3]); }
__device__ __forceinline__ void wg_barrier() {
  asm volatile("s_waitcnt lgkmcnt(0)\n\ts_barrier" ::: "memory");
}
template <int N, bool CLEAN> __device__ __forceinline__ bool untag(u32x4 (&v)[N], u32 tg) {
  u32 bad = 0;
#pragma unroll
  for (int k = 0; k < N; ++k) {
    if (CLEAN) { v[k] ^= tg; bad |= or4(v[k]); }
    else bad |= __builtin_amdgcn_bitop3_b32(v[k][0], v[k][1], tg, 0x7e) | __builtin_amdgcn_bitop3_b32(v[k][2], v[k][3], tg, 0x7e);
  }
  return __all((bad & 0xffu) == 0);
}
template <int N, bool CLEAN = true, typename F>
__device__ __forceinline__ void gather(__amdgpu_buffer_rsrc_t rs, const int (&off)[N], u32 tg, u32x4 (&v)[N], F&& behind) {
#pragma unroll
  for (int k = 0; k < N; ++k) v[k] = ldsc1(rs, off[k]);
  SCHED();
  behind();
  SCHED();
  if (__builtin_expect(!untag<N, CLEAN>(v, tg), 0)) {
    for (u32 spins = 0; spins < SPIN_CAP; ++spins) {
      asm volatile("" ::: "memory");
#pragma unroll
      for (int k = 0; k < N; ++k) v[k] = ldsc1(rs, off[k]);
      if (untag<N, CLEAN>(v, tg)) break;
    }
  }
}
__device__ __forceinline__ f32x2 lo2(f32x4 v) { return __builtin_shufflevector(v, v, 0, 1); }
__device__ __forceinline__ f32x2 hi2(f32x4 v) { return __builtin_shufflevector(v, v, 2, 3); }
__device__ __forceinline__ float sum8(const float (&y)[8]) {
  f32x2 t = (f32x2{y[0], y[1]} + f32x2{y[2], y[3]}) + (f32x2{y[4], y[5]} + f32x2{y[6], y[7]});
  PIN(t);
  return t[0] + t[1];
}
}

#define STAMP(i) do { if ((i) == 0) t0_ = __builtin_amdgcn_s_memrealtime(); else if ((i) == 1) { while (__builtin_amdgcn_s_memrealtime() - t0_ < 900) __builtin_amdgcn_s_sleep(1); } } while (0)
#define CST(i) do {} while (0)
#define KDELAY {0, 18, 18, 18, 18, 18, 18, 18}
constexpr int kDelay[8] = KDELAY;
#define DLY(i) do { if (kDelay[i] > 0) asm volatile("s_sleep %0" :: "i"(kDelay[i])); } while (0)
#define TUNE_PARAM
#define TUNE_ARG
#define QSHIFT 1
#define LORD 3
#define LBAR 7
#define TEST 0
#define WOPOS 0
#define W1POS 1
#define OSHIFT 1

__global__ void __launch_bounds__(512, 2)
fused_decode(const uint16_t* __restrict__ x, const uint16_t* __restrict__ kc, const uint16_t* __restrict__ vc,
             const uint16_t* __restrict__ wq, const uint16_t* __restrict__ wo, const uint16_t* __restrict__ w1,
             const uint16_t* __restrict__ b1, const uint16_t* __restrict__ w2, const uint16_t* __restrict__ b2,
             uint16_t* __restrict__ out, char* ws TUNE_PARAM) {
  __shared__ __attribute__((aligned(16))) float s_ow[64 * 64];
  __shared__ __attribute__((aligned(16))) float s_ml[4 * 16];
  __shared__ __attribute__((aligned(16))) uint16_t s_q[512];
  __shared__ __attribute__((aligned(16))) uint16_t s_x1[512 + 8];
  __shared__ __attribute__((aligned(16))) float s_hp[2 * 16 * 32];
  __shared__ __attribute__((aligned(16))) float s_st[32];
  __shared__ __attribute__((aligned(16))) uint16_t s_a[8 * 64];
  __shared__ __attribute__((aligned(16))) float s_xp[8 * 64];
  __shared__ __attribute__((aligned(16))) float s_wp[32 * 64];

  const int tid = threadIdx.x, bid = blockIdx.x;
  const int w = __builtin_amdgcn_readfirstlane(tid >> 6);
  const int l = tid & 63, i16 = l & 15, kg = l >> 4;
  const int g = bid & 7, s = (bid >> 3) & 7, b = bid >> 6;
  const int bg = b * 8 + g;
  const bool bit3 = (l & 8) != 0;
  const bool mlp_on = s < 7;
  const int f0 = g * 224 + (mlp_on ? s : 6) * 32;
  const __amdgpu_buffer_rsrc_t rs = __builtin_amdgcn_make_buffer_rsrc((void*)ws, 0, WS_END, 0x00020000);
  unsigned long long t0_;
  STAMP(0);
  const int gq = (g + QSHIFT) & 7, go = (g + OSHIFT) & 7;

  const u32 ep = __builtin_amdgcn_raw_buffer_load_b32(rs, EP_OFF + b * 256, 0, AUX_SC1);
  const u32x4 xfull = *(const u32x4*)(x + b * 512 + l * 8);
  const u32x4 xa0 = *(const u32x4*)(x + b * 512 + s * 64 + (i16 & 1) * 32 + kg * 8);
  const int e8 = (l >> 5) * 4 + ((l >> 4) & 1) * 2 + ((l >> 3) & 1);
  const int c8 = (l & 7) * 8 + e8;
  u32x4 wqr[8], kr[8], vr[8], wor[8];
  u32x2 w1r[8], w2r[8];
  {
    const uint16_t* p = wq + (size_t)(s * 64 + ((l >> 3) & 1) * 32 + kg * 8) * 4096 + gq * 512 + w * 64 + (l & 7) * 8;
#pragma unroll
    for (int r = 0; r < 8; ++r) wqr[r] = *(const u32x4*)(p + r * 4096);
  }
  __builtin_amdgcn_sched_barrier(0);
  asm volatile("s_barrier" ::: "memory");
  __builtin_amdgcn_sched_barrier(0);
  const size_t kvbase = ((size_t)bg * SK + (SK - WIN) + s * 256 + w * 32) * 128;
  {
    const uint16_t* p = kc + kvbase + (i16 >> 2) * 8 * 128 + kg * 32 + (i16 & 3) * 8;
#pragma unroll
    for (int m = 0; m < 8; ++m) kr[m] = __builtin_nontemporal_load((const u32x4*)(p + m * 128));
  }
  auto load_v = [&](int r0, int r1) {
    const uint16_t* p = vc + kvbase + kg * 8 * 128 + i16 * 8;
#pragma unroll
    for (int r = r0; r < r1; ++r) vr[r] = __builtin_nontemporal_load((const u32x4*)(p + r * 128));
  };
  if (LORD == 0) load_v(0, 8);
  __builtin_amdgcn_sched_barrier(0);
  if (LBAR & 1) asm volatile("s_barrier" ::: "memory");
  const float b1v = asf((u32)b1[f0 + (l & 31)] << 16);
  const float b2a = asf((u32)b2[s * 64 + l] << 16);
  const float b2f = asf((u32)b2[s * 64 + g * 8 + e8] << 16);
  u32 xres = (u32)x[b * 512 + w * 64 + c8] << 16;
  __builtin_amdgcn_sched_barrier(0);
  const u32 tn = __builtin_amdgcn_readfirstlane(ep) + 1u;
  const u32 t8 = tn & 0xffu, t16 = tn & 0xffffu;
  u32 t8v = t8;
  PIN(t8v);

  u32x4 ya0;
  {
    float xv[8];
#pragma unroll
    for (int j = 0; j < 4; ++j) { xv[2 * j] = asf(xfull[j] << 16); xv[2 * j + 1] = asf(xfull[j] & 0xffff0000u); }
    float sm = 0.f;
#pragma unroll
    for (int j = 0; j < 8; ++j) sm += xv[j];
    const float mean = wave_sum(sm) * (1.f / 512.f);
    float sq = 0.f;
#pragma unroll
    for (int j = 0; j < 8; ++j) sq += (xv[j] - mean) * (xv[j] - mean);
    const float mb = rbf(mean);
    const float rsd = __builtin_amdgcn_rcpf(rbf(__builtin_amdgcn_sqrtf(rbf(rbf(wave_sum(sq) * (1.f / 512.f)) + EPS))));
#pragma unroll
    for (int j = 0; j < 4; ++j) {
      const u32 c = pk_bf16(asf(xa0[j] << 16) - mb, asf(xa0[j] & 0xffff0000u) - mb);
      ya0[j] = pk_bf16(asf(c << 16) * rsd, asf(c & 0xffff0000u) * rsd);
    }
  }
  u32x4 wqt[8];
  tr8<4>(wqr, wqt);
  u32x4 ones = {0x3f803f80u, 0x3f803f80u, 0x3f803f80u, 0x3f803f80u};
  PIN(ones);
  auto none = []() {};
  const f32x4 zero4 = {0.f, 0.f, 0.f, 0.f};

  auto s1_core = [&](u32x4 a, int dst, bool on) {
    f32x4 d[8];
#pragma unroll
    for (int c = 0; c < 8; ++c) d[c] = mfma(a, wqt[c], zero4);
#pragma unroll
    for (int c = 0; c < 8; ++c) PIN(d[c]);
    float keep[4], send[4];
#pragma unroll
    for (int j = 0; j < 4; ++j) send[j] = bit3 ? d[j][1] : d[4 + j][0];
#pragma unroll
    for (int j = 0; j < 4; ++j) keep[j] = bit3 ? d[4 + j][1] : d[j][0];
    SCHED();
    u32x4 o;
#pragma unroll
    for (int j = 0; j < 4; ++j) { float t = keep[j] + dpp<ROR8>(send[j]); PIN(t); o[j] = tag8(t, t8v); }
    if (on) stsc1(rs, dst, o);
  };
  if (tid < 4) ((u32*)(s_x1 + 512))[tid] = 0x3f803f80u;
  s1_core(bit3 ? ones : ya0, (kg == 0 ? QP_OFF : CS_OFF) + ((b * 8 + gq) * 8 + s) * 2048 + w * 256 + i16 * 16, (kg & 1) == 0);
  STAMP(1);
  __builtin_amdgcn_sched_barrier(0);
  if (LBAR & 2) asm volatile("s_barrier" ::: "memory");
  __builtin_amdgcn_sched_barrier(0);
  if (LORD == 2) load_v(0, 4);
  if (LORD == 3) load_v(0, 8);
  __builtin_amdgcn_sched_barrier(0);
  if (LBAR & 4) asm volatile("s_barrier" ::: "memory");
  __builtin_amdgcn_sched_barrier(0);
  auto load_wo = [&]() {
    const uint16_t* p = wo + (size_t)(go * 512 + (((l >> 3) & 1) * 4 + kg) * 64 + w) * 512 + s * 64 + (l & 7) * 8;
#pragma unroll
    for (int r = 0; r < 8; ++r) wor[r] = *(const u32x4*)(p + r * 8 * 512);
  };
  auto load_w1 = [&]() {
    const uint16_t* p = w1 + (size_t)(w * 64 + ((l >> 3) & 1) * 32 + kg * 8) * FF + f0 + (l & 7) * 4;
#pragma unroll
    for (int r = 0; r < 8; ++r) w1r[r] = *(const u32x2*)(p + r * FF);
  };
  auto load_w2 = [&]() {
    const uint16_t* p2 = w2 + (size_t)(f0 + kg * 8) * 512 + w * 64 + i16 * 4;
#pragma unroll
    for (int r = 0; r < 8; ++r) w2r[r] = *(const u32x2*)(p2 + r * 512);
  };

  const int xo = X2_OFF + bg * 2048 + w * 256 + c8 * 4;
  u32x4 vt[8], wot[8], w1t[4], w2t[4];
  float csq = 0.f, cs1 = 0.f;
  auto red8 = [&](const float (&v)[8]) {
    const float u0 = rs32(v[0], v[4]), u1 = rs32(v[1], v[5]), u2 = rs32(v[2], v[6]), u3 = rs32(v[3], v[7]);
    return rs8(rs16(u0, u2), rs16(u1, u3), bit3);
  };

#pragma unroll
  for (int layer = 0; layer < 2; ++layer) {
    {
      const int qo = (bg * 8 + (l >> 3)) * 2048 + w * 256 + (l & 7) * 16;
      int od = OP_OFF + ((layer * 32 + bg) * 8 + s) * 2048 + w * 256 + ((l >> 2) & 1) * 128 + (l >> 3) * 16 + (l & 3) * 4;
      PIN(od);
      float a[8], mean = 0.f, rstd = 1.f;
      DLY(layer * 4);
      if (layer == 0) {
        u32x4 v[4];
        const int off[4] = {QP_OFF + qo, QP_OFF + qo + 128, CS_OFF + qo, CS_OFF + qo + 128};
        gather<4>(rs, off, t8, v, [&]() { if (WOPOS == 0) load_wo(); });
        STAMP(2);
        float c[8];
#pragma unroll
        for (int j = 0; j < 4; ++j) { a[j] = asf(v[0][j]); a[4 + j] = asf(v[1][j]); c[j] = asf(v[2][j]); c[4 + j] = asf(v[3][j]); }
        csq = red8(c);
      } else {
        u32x4 v[3];
        const int off[3] = {ST_OFF + (bg * 8 + (l >> 3)) * 128 + (l & 7) * 16, QP_OFF + 256 * 2048 + qo, QP_OFF + 256 * 2048 + qo + 128};
        gather<3>(rs, off, t8, v, [&]() { xres = __builtin_amdgcn_raw_buffer_load_b32(rs, xo, 0, AUX_SC1); });
        STAMP(10);
        CST(0);
#pragma unroll
        for (int j = 0; j < 4; ++j) { a[j] = asf(v[1][j]); a[4 + j] = asf(v[2][j]); }
        float u = rs32(asf(v[0][0]) + asf(v[0][1]), asf(v[0][2]) + asf(v[0][3]));
        u = rs16(u, u);
        u += dpp<ROR8>(u);
        auto r = __builtin_amdgcn_permlane32_swap(asu(u), asu(u), false, false);
        mean = asf(r[0]) * (1.f / 512.f);
        rstd = __builtin_amdgcn_rsqf(fmx(asf(r[1]) * (1.f / 512.f) - mean * mean, 0.f) + EPS);
      }
      const float qv = layer == 0 ? red8(a) : (red8(a) - mean * csq) * rstd;
      s_q[w * 64 + c8] = (uint16_t)(pk_bf16(qv, qv) & 0xffffu);
      CST(1);
      wg_barrier();
      CST(2);
      const u32x4 qf = *(const u32x4*)(s_q + (i16 >> 2) * 128 + kg * 32 + (i16 & 3) * 8);
      f32x4 d[8];
#pragma unroll
      for (int m = 0; m < 8; ++m) d[m] = mfma(kr[m], qf, zero4);
      SCHED();
      float sv[8];
#pragma unroll
      for (int m = 0; m < 8; ++m) { sv[m] = d[m][0] + dpp<0x55>(d[m][1]); PIN(sv[m]); }
#pragma unroll
      for (int m = 0; m < 8; ++m) { sv[m] += dpp<0xAA>(d[m][2]); PIN(sv[m]); }
#pragma unroll
      for (int m = 0; m < 8; ++m) { sv[m] += dpp<0xFF>(d[m][3]); PIN(sv[m]); }
      if (layer == 0) tr8<4>(vr, vt);
      CST(3);
      if (layer == 0) {
#pragma unroll
        for (int j = 0; j < 4; ++j) {
          const u32 c = pk_bf16(sv[2 * j], sv[2 * j + 1]);
          const f32x2 t = f32x2{asf(c << 16), asf(c & 0xffff0000u)} * 0.08838834764831845f;
          const u32 e = pk_bf16(t[0], t[1]);
          sv[2 * j] = asf(e << 16); sv[2 * j + 1] = asf(e & 0xffff0000u);
        }
      }
      const float sc = (layer == 0 ? 1.f : 0.08838834764831845f) * 1.4426950408889634f;
      float m = fmx(fmx(fmx(fmx(sv[0], sv[1]), sv[2]), fmx(fmx(sv[3], sv[4]), sv[5])), fmx(sv[6], sv[7]));
      m = mx32(mx16(m)) * sc;
      u32x4 pa;
#pragma unroll
      for (int j = 0; j < 4; ++j) {
        const f32x2 e = f32x2{sv[2 * j], sv[2 * j + 1]} * sc - m;
        pa[j] = pk_bf16(__builtin_amdgcn_exp2f(e[0]), __builtin_amdgcn_exp2f(e[1]));
      }
      CST(4);
      f32x4 dl = mfma(pa, ones, zero4);
      SCHED();
#pragma unroll
      for (int c2 = 0; c2 < 8; ++c2) d[c2] = mfma(pa, vt[c2], zero4);
      PIN(dl);
#pragma unroll
      for (int c2 = 0; c2 < 8; ++c2) PIN(d[c2]);
      if (i16 == 4 * kg) { s_ml[kg * 16 + w] = m; s_ml[kg * 16 + 8 + w] = dl[0]; }
#pragma unroll
      for (int c2 = 0; c2 < 8; ++c2) s_ow[(w * 8 + c2) * 64 + l] = d[c2][0];
      CST(5);
      wg_barrier();
      CST(6);
      const f32x4 m0 = *(const f32x4*)(s_ml + kg * 16), m1 = *(const f32x4*)(s_ml + kg * 16 + 4);
      const f32x4 l0 = *(const f32x4*)(s_ml + kg * 16 + 8), l1 = *(const f32x4*)(s_ml + kg * 16 + 12);
      float ov[8];
#pragma unroll
      for (int u = 0; u < 8; ++u) ov[u] = s_ow[(u * 8 + w) * 64 + l];
      const float M = fmx(fmx(fmx(fmx(m0[0], m0[1]), m0[2]), fmx(fmx(m0[3], m1[0]), m1[1])), fmx(m1[2], m1[3]));
      const f32x2 mp[4] = {lo2(m0), hi2(m0), lo2(m1), hi2(m1)}, lp[4] = {lo2(l0), hi2(l0), lo2(l1), hi2(l1)};
      f32x2 L2, o2;
#pragma unroll
      for (int u = 0; u < 4; ++u) {
        const f32x2 e = mp[u] - M;
        const f32x2 c = {__builtin_amdgcn_exp2f(e[0]), __builtin_amdgcn_exp2f(e[1])};
        const f32x2 ou = {ov[2 * u], ov[2 * u + 1]};
        L2 = u ? c * lp[u] + L2 : c * lp[u];
        o2 = u ? c * ou + o2 : c * ou;
      }
      PIN(L2); PIN(o2);
      const float L = L2[0] + L2[1];
      const float on = (o2[0] + o2[1]) * __builtin_amdgcn_rcpf(L);
      const float lw = M + __builtin_amdgcn_logf(L);
      __builtin_amdgcn_raw_buffer_store_b32(__builtin_amdgcn_perm(pk_bf16(on, on), asu(lw), 0x0706000cu | ((l & 3) << 8)) | t8, rs, od, 0, AUX_SC1);
      STAMP(3 + layer * 8);
      CST(7);
    }
    {
      if (layer == 0 && WOPOS == 1) { SCHED(); load_wo(); SCHED(); }
      const int oo = OP_OFF + ((layer * 32 + b * 8 + go) * 8 + (l & 7)) * 2048 + w * 256 + (((l >> 3) & 1) * 4 + kg) * 16;
      const int off[2] = {oo, oo + 128};
      int wd = WP_OFF + (layer * 4 + b) * 16384 + g * 2048 + s * 256 + (l & 1) * 128 + (i16 >> 1) * 16 + (l >> 5) * 8 + (kg & 1) * 4;
      PIN(wd);
      u32x4 v[2];
      DLY(1 + layer * 4);
      if (layer == 0) gather<2, false>(rs, off, t8, v, [&]() {
        if (W1POS == 0) { load_w1(); SCHED(); }
        tr8<4>(wor, wot);
#pragma unroll
        for (int c = 0; c < 8; ++c) PIN(wot[c]);
      }); else gather<2, false>(rs, off, t8, v, [&]() {
        if (__builtin_expect(!__all(((xres ^ t16) & 0xffffu) == 0), 0)) {
          for (u32 spins = 0; spins < SPIN_CAP; ++spins) {
            asm volatile("" ::: "memory");
            xres = __builtin_amdgcn_raw_buffer_load_b32(rs, xo, 0, AUX_SC1);
            if (__all(((xres ^ t16) & 0xffffu) == 0)) break;
          }
        }
        xres &= 0xffff0000u;
        PIN(xres);
      });
      STAMP(4 + layer * 8);
      CST(8);
      const float lw = asf(__builtin_amdgcn_perm(__builtin_amdgcn_perm(v[0][3], v[0][2], 0x05010000u), __builtin_amdgcn_perm(v[0][1], v[0][0], 0x00000501u), 0x07060100u));
      float M = fmx(lw, dpp<QX1>(lw)); M = fmx(M, dpp<QX2>(M)); M = fmx(M, dpp<HMIR>(M));
      float cw = __builtin_amdgcn_exp2f(lw - M);
      float T = cw + dpp<QX1>(cw); T += dpp<QX2>(T); T += dpp<HMIR>(T);
      cw *= __builtin_amdgcn_rcpf(T);
      u32x4 a;
#pragma unroll
      for (int j = 0; j < 4; ++j)
        a[j] = pk_bf16(asf(v[j >> 1][(j & 1) * 2] & 0xffff0000u) * cw, asf(v[j >> 1][(j & 1) * 2 + 1] & 0xffff0000u) * cw);
      f32x4 d[8];
#pragma unroll
      for (int c = 0; c < 8; ++c) d[c] = mfma(a, wot[c], zero4);
      f32x4 z[2];
#pragma unroll
      for (int c = 0; c < 8; ++c) { f32x2 t = lo2(d[c]) + hi2(d[c]); PIN(t); float r = t[0] + t[1]; PIN(r); z[c >> 2][c & 3] = r; }
      if (((l >> 3) & 1) == (l >> 5)) {
        *(f32x4*)(s_wp + (w * 4 + kg) * 64 + (l & 7) * 8) = z[0];
        *(f32x4*)(s_wp + (w * 4 + kg) * 64 + (l & 7) * 8 + 4) = z[1];
      }
      CST(9);
      wg_barrier();
      CST(10);
      if (w == 0) {
        f32x4 p[8];
#pragma unroll
        for (int k = 0; k < 8; ++k) p[k] = *(const f32x4*)(s_wp + (kg * 8 + k) * 64 + i16 * 4);
        const f32x4 acc = ((p[0] + p[1]) + (p[2] + p[3])) + ((p[4] + p[5]) + (p[6] + p[7]));
        const float t = rs16(rs32(acc[0], acc[2]), rs32(acc[1], acc[3]));
        __builtin_amdgcn_raw_buffer_store_b32(tag8(t, t8v), rs, wd, 0, AUX_SC1);
      }
      STAMP(5 + layer * 8);
      CST(11);
    }
    {
      if (layer == 0 && W1POS == 1) { SCHED(); load_w1(); SCHED(); }
      const int wo_ = WP_OFF + (layer * 4 + b) * 16384 + (l >> 3) * 2048 + w * 256 + (l & 7) * 16;
      const int off[2] = {wo_, wo_ + 128};
      int md = MP_OFF + (((layer * 4 + b) * 8 + w) * 56 + g * 7 + s) * 256 + l * 16;
      PIN(md);
      u32x4 v[2];
      DLY(2 + layer * 4);
      if (layer == 0) gather<2>(rs, off, t8, v, [&]() {
        load_w2();
        SCHED();
        tr8<2>(w1r, w1t);
#pragma unroll
        for (int c = 0; c < 4; ++c) PIN(w1t[c]);
      }); else gather<2>(rs, off, t8, v, none);
      STAMP(6 + layer * 8);
      CST(12);
      float y[8];
#pragma unroll
      for (int j = 0; j < 8; ++j) y[j] = asf(v[j >> 2][j & 3]);
      const float x1 = rbf(red8(y)) + asf(xres);
      s_x1[w * 64 + c8] = (uint16_t)(pk_bf16(x1, x1) & 0xffffu);
      asm volatile("" ::: "memory");
      const int xf = w * 64 + (i16 & 1) * 32 + kg * 8;
      const u32x4 am = *(const u32x4*)(s_x1 + (bit3 ? 512 : xf));
      const u32x4 bx = *(const u32x4*)(s_x1 + xf);
      f32x4 d[4];
#pragma unroll
      for (int c = 0; c < 4; ++c) d[c] = mfma(am, w1t[c], zero4);
      f32x4 gr = mfma(am, bx, zero4);
#pragma unroll
      for (int c = 0; c < 4; ++c) PIN(d[c]);
      if (l < 16 || (layer == 0 && kg == 2)) {
        const f32x4 hv = bit3 ? f32x4{d[0][1], d[1][1], d[2][1], d[3][1]} : f32x4{d[0][0], d[1][0], d[2][0], d[3][0]};
        *(f32x4*)(s_hp + (l >> 5) * 512 + (w * 2 + ((l >> 3) & 1)) * 32 + (l & 7) * 4) = hv;
      }
      if ((l & 30) == 0) s_st[(l >> 5) * 16 + w * 2 + (l & 1)] = (l & 1) ? gr[1] : gr[0];
      if (layer == 0) tr8<2>(w2r, w2t);
      wg_barrier();
      if (mlp_on) {
        float st = s_st[l & 31];
        float hp8[8];
#pragma unroll
        for (int p = 0; p < 8; ++p) hp8[p] = s_hp[((l >> 5) * 8 + p) * 32 + (l & 31)];
        if (layer == 0) {
          float ac = 0.f;
#pragma unroll
          for (int p = 0; p < 8; ++p) ac += s_hp[512 + ((l >> 5) * 8 + p) * 32 + (l & 31)];
          cs1 = rs32(ac, ac);
        }
        st += dpp<ROR8>(st); st += dpp<ROR4>(st); st += dpp<ROR2>(st); st += dpp<ROR1>(st);
        float acc = sum8(hp8);
        acc = rs32(acc, acc);
        auto r = __builtin_amdgcn_permlane16_swap(asu(st), asu(st), false, false);
        const float mu = asf(r[1]) * (1.f / 512.f);
        const float var = fmx(asf(r[0]) * (1.f / 512.f) - mu * mu, 0.f);
        const float r12 = __builtin_amdgcn_rsqf(var + EPS * (var + EPS));
        const float hp = (acc - mu * cs1) * r12 + b1v;
        const float u = hp * (-2.302208198144325f - 0.1029432395800235f * (hp * hp));
        const float hv = hp * __builtin_amdgcn_rcpf(1.f + __builtin_amdgcn_exp2f(u));
        s_a[w * 64 + l] = (uint16_t)(pk_bf16(hv, hv) & 0xffffu);
        asm volatile("" ::: "memory");
        const u32x4 ha = *(const u32x4*)(s_a + w * 64 + kg * 8);
        f32x4 e[4];
#pragma unroll
        for (int c = 0; c < 4; ++c) e[c] = mfma(ha, w2t[c], zero4);
#pragma unroll
        for (int c = 0; c < 4; ++c) PIN(e[c]);
        const u32x4 o = {tag8(e[0][0], t8v), tag8(e[1][0], t8v), tag8(e[2][0], t8v), tag8(e[3][0], t8v)};
        if (l < 16) stsc1(rs, md, o);
      }
      STAMP(7 + layer * 8);
      CST(13);
    }
    if (layer == 0) {
      float xr = asf((u32)s_x1[s * 64 + l] << 16);
      PIN(xr);
      const int mo = MP_OFF + ((b * 8 + s) * 56 + w * 7 + kg) * 256 + i16 * 16;
      const int off[2] = {mo, kg < 3 ? mo + 1024 : mo};
      float two = kg < 3 ? 1.f : 0.f;
      int qd = QP_OFF + ((32 + b * 8 + gq) * 8 + s) * 2048 + w * 256 + i16 * 16;
      int sd = ST_OFF + ((b * 8 + gq) * 8 + s) * 128 + (l & 7) * 16 + (l >> 5) * 8;
      int xpi = w * 64 + i16 * 4 + (l >> 5) * 2 + (kg & 1), sai = w * 64 + (i16 & 1) * 32 + kg * 8;
      float b2p = b2a;
      PIN(two); PIN(qd); PIN(sd); PIN(xpi); PIN(sai); PIN(b2p);
      u32x4 v[2];
      DLY(3);
      gather<2>(rs, off, t8, v, none);
      STAMP(8);
      float y[8];
#pragma unroll
      for (int j = 0; j < 4; ++j) y[j] = asf(v[0][j]) + two * asf(v[1][j]);
      s_xp[xpi] = rs16(rs32(y[0], y[2]), rs32(y[1], y[3]));
      wg_barrier();
#pragma unroll
      for (int u = 0; u < 8; ++u) y[u] = s_xp[u * 64 + l];
      const float x2f = rbf(rbf(sum8(y)) + b2p) + xr;
      const u32 x2 = pk_bf16(x2f, x2f);
      s_a[w * 64 + l] = (uint16_t)(x2 & 0xffffu);
      asm volatile("" ::: "memory");
      const u32x4 a = *(const u32x4*)(s_a + sai);
      if (w == 6) {
        const float xv = asf(x2 & 0xffff0000u);
        float u = rs32(xv, xv * xv);
        u = rs16(u, u);
        u += dpp<ROR8>(u); u += dpp<ROR4>(u); u += dpp<ROR2>(u); u += dpp<ROR1>(u);
        const float hi = asf(asu(u) & 0xffffff00u);
        const u32x2 o = {asu(hi) | t8, tag8(u - hi, t8v)};
        if ((l & 24) == 0) __builtin_amdgcn_raw_buffer_store_b64(o, rs, sd, 0, AUX_SC1);
      }
      s1_core(a, qd, l < 16);
      if (w == 7) __builtin_amdgcn_raw_buffer_store_b32((x2 & 0xffff0000u) | t16, rs, X2_OFF + (b * 8 + gq) * 2048 + s * 256 + l * 4, 0, AUX_SC1);
      STAMP(9);
    } else if (w == 0) {
      const int oc = s * 64 + g * 8 + e8;
      const float xr = asf((u32)s_x1[oc] << 16);
      const int mo = MP_OFF + (((4 + b) * 8 + s) * 56 + (l < 56 ? l : 0)) * 256 + g * 32;
      const int off[2] = {mo, mo + 16};
      u32x4 v[2];
      DLY(7);
      gather<2>(rs, off, t8, v, none);
      STAMP(16);
      float y[8];
#pragma unroll
      for (int j = 0; j < 8; ++j) y[j] = l < 56 ? asf(v[j >> 2][j & 3]) : 0.f;
      float t = red8(y);
      t += dpp<QX1>(t); t += dpp<QX2>(t); t += dpp<HMIR>(t);
      const float r = rbf(rbf(t) + b2f) + xr;
      if ((l & 7) == 0) out[b * 512 + oc] = (uint16_t)(pk_bf16(r, r) & 0xffffu);
      if (l == 0 && g == 0 && s == 0) __builtin_amdgcn_raw_buffer_store_b32(tn, rs, EP_OFF + b * 256, 0, AUX_SC1);
      STAMP(17);
    }
  }
}

extern "C" void kernel_launch(void* const* d_in, const int* in_sizes, int n_in, void* d_out, int out_size, void* d_ws,
                              size_t ws_size, hipStream_t stream) {
  (void)in_sizes; (void)n_in; (void)out_size; (void)ws_size;
  fused_decode<<<256, 512, 0, stream>>>((const uint16_t*)d_in[0], (const uint16_t*)d_in[1], (const uint16_t*)d_in[2],
                                        (const uint16_t*)d_in[3], (const uint16_t*)d_in[4], (const uint16_t*)d_in[6],
                                        (const uint16_t*)d_in[7], (const uint16_t*)d_in[8], (const uint16_t*)d_in[9],
                                        (uint16_t*)d_out, (char*)d_ws TUNE_ARG);
}
```

```cpp
#include <hip/hip_runtime.h>
#include <hip/hip_bf16.h>
#include <stdint.h>

#define DIAG 0

typedef unsigned int u32;
typedef __attribute__((ext_vector_type(4))) unsigned int u32x4;
typedef __attribute__((ext_vector_type(2))) unsigned int u32x2;
typedef __attribute__((ext_vector_type(4))) float f32x4;
typedef __attribute__((ext_vector_type(2))) float f32x2;
typedef __attribute__((ext_vector_type(8))) __bf16 bf16x8;
typedef __attribute__((ext_vector_type(2))) __bf16 bf16x2;

namespace {
constexpr int SK = 8192, WIN = 2048, FF = 1792;
constexpr float EPS = 1e-5f;
constexpr u32 SPIN_CAP = 1u << 17;
constexpr int EP_OFF = 0;
constexpr int QP_OFF = 4096;
constexpr int CS_OFF = QP_OFF + 2 * 256 * 2048;
constexpr int X2_OFF = CS_OFF + 256 * 2048;
constexpr int OP_OFF = X2_OFF + 32 * 2048;
constexpr int ST_OFF = OP_OFF + 2 * 256 * 2048;
constexpr int WP_OFF = ST_OFF + 256 * 128;
constexpr int MP_OFF = WP_OFF + 2 * 4 * 16384;
constexpr int WS_END = MP_OFF + 2 * 4 * 8 * 56 * 256;
constexpr int DG_OFF = WS_END;
constexpr int AUX_SC1 = 16;

__device__ __forceinline__ float asf(u32 x) { return __builtin_bit_cast(float, x); }
__device__ __forceinline__ u32 asu(float x) { return __builtin_bit_cast(u32, x); }
__device__ __forceinline__ u32 pk_bf16(float lo, float hi) {
  f32x2 v = {lo, hi};
  return __builtin_bit_cast(u32, __builtin_convertvector(v, bf16x2));
}
__device__ __forceinline__ float rbf(float x) {
  return asf(pk_bf16(x, x) & 0xffff0000u);
}
__device__ __forceinline__ float fmx(float a, float b) { return __builtin_elementwise_maximum(a, b); }
#define PIN(x) asm volatile("" : "+v"(x))
#define SCHED() __builtin_amdgcn_sched_barrier(0)
template <int CTRL> __device__ __forceinline__ float dpp(float x) {
  return asf(__builtin_amdgcn_update_dpp(0, asu(x), CTRL, 0xf, 0xf, true));
}
constexpr int ROR8 = 0x128, ROR4 = 0x124, ROR2 = 0x122, ROR1 = 0x121, QX1 = 0xB1, QX2 = 0x4E, HMIR = 0x141;
__device__ __forceinline__ float rs32(float a, float b) {
  auto r = __builtin_amdgcn_permlane32_swap(asu(a), asu(b), false, false);
  return asf(r[0]) + asf(r[1]);
}
__device__ __forceinline__ float rs16(float a, float b) {
  auto r = __builtin_amdgcn_permlane16_swap(asu(a), asu(b), false, false);
  return asf(r[0]) + asf(r[1]);
}
__device__ __forceinline__ float rs8(float a, float b, bool bit3) {
  return (bit3 ? b : a) + dpp<ROR8>(bit3 ? a : b);
}
__device__ __forceinline__ float mx32(float a) {
  auto r = __builtin_amdgcn_permlane32_swap(asu(a), asu(a), false, false);
  return fmx(asf(r[0]), asf(r[1]));
}
__device__ __forceinline__ float mx16(float a) {
  auto r = __builtin_amdgcn_permlane16_swap(asu(a), asu(a), false, false);
  return fmx(asf(r[0]), asf(r[1]));
}
__device__ __forceinline__ float wave_sum(float a) {
  a += dpp<ROR8>(a); a += dpp<ROR4>(a); a += dpp<ROR2>(a); a += dpp<ROR1>(a);
  a = rs16(a, a); return rs32(a, a);
}
__device__ __forceinline__ f32x4 mfma(u32x4 a, u32x4 b, f32x4 c) {
  return __builtin_amdgcn_mfma_f32_16x16x32_bf16(__builtin_bit_cast(bf16x8, a), __builtin_bit_cast(bf16x8, b), c, 0, 0, 0);
}
template <int NW, typename V> __device__ __forceinline__ void tr8(const V (&r)[8], u32x4 (&t)[2 * NW]) {
#pragma unroll
  for (int c = 0; c < NW; ++c)
#pragma unroll
    for (int j = 0; j < 4; ++j) {
      t[2 * c][j] = __builtin_amdgcn_perm(r[2 * j + 1][c], r[2 * j][c], 0x05040100u);
      t[2 * c + 1][j] = __builtin_amdgcn_perm(r[2 * j + 1][c], r[2 * j][c], 0x07060302u);
    }
}
__device__ __forceinline__ u32x4 ldsc1(__amdgpu_buffer_rsrc_t rs, int off) {
  return __builtin_amdgcn_raw_buffer_load_b128(rs, off, 0, AUX_SC1);
}
__device__ __forceinline__ void stsc1(__amdgpu_buffer_rsrc_t rs, int off, u32x4 v) {
  __builtin_amdgcn_raw_buffer_store_b128(v, rs, off, 0, AUX_SC1);
}
__device__ __forceinline__ u32 tag8(float v, u32 t8) { return (asu(v) & 0xffffff00u) | t8; }
__device__ __forceinline__ u32 or4(u32x4 v) { return (v[0] | v[1]) | (v[2] | v[3]); }
__device__ __forceinline__ void wg_barrier() {
  asm volatile("s_waitcnt lgkmcnt(0)\n\ts_barrier" ::: "memory");
}
template <int N, bool CLEAN> __device__ __forceinline__ bool untag(u32x4 (&v)[N], u32 tg) {
  u32 bad = 0;
#pragma unroll
  for (int k = 0; k < N; ++k) {
    if (CLEAN) { v[k] ^= tg; bad |= or4(v[k]); }
    else bad |= __builtin_amdgcn_bitop3_b32(v[k][0], v[k][1], tg, 0x7e) | __builtin_amdgcn_bitop3_b32(v[k][2], v[k][3], tg, 0x7e);
  }
  return __all((bad & 0xffu) == 0);
}
template <int N, bool CLEAN = true, typename F>
__device__ __forceinline__ void gather(__amdgpu_buffer_rsrc_t rs, const int (&off)[N], u32 tg, u32x4 (&v)[N], F&& behind) {
#pragma unroll
  for (int k = 0; k < N; ++k) v[k] = ldsc1(rs, off[k]);
  SCHED();
  behind();
  SCHED();
  if (__builtin_expect(!untag<N, CLEAN>(v, tg), 0)) {
    for (u32 spins = 0; spins < SPIN_CAP; ++spins) {
      asm volatile("" ::: "memory");
#pragma unroll
      for (int k = 0; k < N; ++k) v[k] = ldsc1(rs, off[k]);
      if (untag<N, CLEAN>(v, tg)) break;
    }
  }
}
__device__ __forceinline__ f32x2 lo2(f32x4 v) { return __builtin_shufflevector(v, v, 0, 1); }
__device__ __forceinline__ f32x2 hi2(f32x4 v) { return __builtin_shufflevector(v, v, 2, 3); }
__device__ __forceinline__ float sum8(const float (&y)[8]) {
  f32x2 t = (f32x2{y[0], y[1]} + f32x2{y[2], y[3]}) + (f32x2{y[4], y[5]} + f32x2{y[6], y[7]});
  PIN(t);
  return t[0] + t[1];
}
}

#define STAMP(i) do {} while (0)
#define CST(i) do {} while (0)
#define KDELAY {0, 18, 18, 18, 18, 18, 18, 18}
constexpr int kDelay[8] = KDELAY;
#define DLY(i) do { if (kDelay[i] > 0) asm volatile("s_sleep %0" :: "i"(kDelay[i])); } while (0)
#define TUNE_PARAM
#define TUNE_ARG
#define QSHIFT 1
#define VSPLIT 4
#define LBAR 7
#define WO_A 0
#define WO_B 2
#define ROT 2
#define W_E 2
#define W2POS 0
#define W1POS 1
#define OSHIFT 1

__global__ void __launch_bounds__(512, 2)
fused_decode(const uint16_t* __restrict__ x, const uint16_t* __restrict__ kc, const uint16_t* __restrict__ vc,
             const uint16_t* __restrict__ wq, const uint16_t* __restrict__ wo, const uint16_t* __restrict__ w1,
             const uint16_t* __restrict__ b1, const uint16_t* __restrict__ w2, const uint16_t* __restrict__ b2,
             uint16_t* __restrict__ out, char* ws TUNE_PARAM) {
  __shared__ __attribute__((aligned(16))) float s_ow[64 * 64];
  __shared__ __attribute__((aligned(16))) float s_ml[4 * 16];
  __shared__ __attribute__((aligned(16))) uint16_t s_q[512];
  __shared__ __attribute__((aligned(16))) uint16_t s_x1[512 + 8];
  __shared__ __attribute__((aligned(16))) float s_hp[2 * 16 * 32];
  __shared__ __attribute__((aligned(16))) float s_st[32];
  __shared__ __attribute__((aligned(16))) uint16_t s_a[8 * 64];
  __shared__ __attribute__((aligned(16))) float s_xp[8 * 64];
  __shared__ __attribute__((aligned(16))) float s_wp[32 * 64];

  const int tid = threadIdx.x, bid = blockIdx.x;
  const int w = __builtin_amdgcn_readfirstlane(tid >> 6);
  const int l = tid & 63, i16 = l & 15, kg = l >> 4;
  const int g = bid & 7, s = (bid >> 3) & 7, b = bid >> 6;
  const int bg = b * 8 + g;
  const bool bit3 = (l & 8) != 0;
  const bool mlp_on = s < 7;
  const int f0 = g * 224 + (mlp_on ? s : 6) * 32;
  const __amdgpu_buffer_rsrc_t rs = __builtin_amdgcn_make_buffer_rsrc((void*)ws, 0, WS_END, 0x00020000);
  STAMP(0);
  const int gq = (g + QSHIFT) & 7, go = (g + OSHIFT) & 7;

  const u32 ep = __builtin_amdgcn_raw_buffer_load_b32(rs, EP_OFF + b * 256, 0, AUX_SC1);
  const u32x4 xfull = *(const u32x4*)(x + b * 512 + l * 8);
  const u32x4 xa0 = *(const u32x4*)(x + b * 512 + s * 64 + (i16 & 1) * 32 + kg * 8);
  const int e8 = (l >> 5) * 4 + ((l >> 4) & 1) * 2 + ((l >> 3) & 1);
  const int c8 = (l & 7) * 8 + e8;
  u32x4 wqr[8], kr[8], vr[8], wor[8];
  u32x2 w1r[8], w2r[8];
  {
    const uint16_t* p = wq + (size_t)(s * 64 + ((l >> 3) & 1) * 32 + kg * 8) * 4096 + gq * 512 + w * 64 + (l & 7) * 8;
#pragma unroll
    for (int r = 0; r < 8; ++r) wqr[r] = *(const u32x4*)(p + r * 4096);
  }
  __builtin_amdgcn_sched_barrier(0);
  asm volatile("s_barrier" ::: "memory");
  __builtin_amdgcn_sched_barrier(0);
  const size_t kvbase = ((size_t)bg * SK + (SK - WIN) + s * 256 + w * 32) * 128;
  {
    const uint16_t* p = kc + kvbase + (i16 >> 2) * 8 * 128 + kg * 32 + (i16 & 3) * 8;
#pragma unroll
    for (int m = 0; m < 8; ++m) kr[m] = __builtin_nontemporal_load((const u32x4*)(p + m * 128));
  }
  auto load_v = [&](int r0, int r1) {
    const uint16_t* p = vc + kvbase + kg * 8 * 128 + i16 * 8;
#pragma unroll
    for (int r = r0; r < r1; ++r) vr[r] = __builtin_nontemporal_load((const u32x4*)(p + r * 128));
  };
  __builtin_amdgcn_sched_barrier(0);
  if (LBAR & 1) asm volatile("s_barrier" ::: "memory");
  const float b1v = asf((u32)b1[f0 + (l & 31)] << 16);
  const float b2a = asf((u32)b2[s * 64 + l] << 16);
  const float b2f = asf((u32)b2[s * 64 + g * 8 + e8] << 16);
  u32 xres = (u32)x[b * 512 + w * 64 + c8] << 16;
  __builtin_amdgcn_sched_barrier(0);
  const u32 tn = __builtin_amdgcn_readfirstlane(ep) + 1u;
  const u32 t8 = tn & 0xffu, t16 = tn & 0xffffu;
  u32 t8v = t8;
  PIN(t8v);

  u32x4 ya0;
  {
    float xv[8];
#pragma unroll
    for (int j = 0; j < 4; ++j) { xv[2 * j] = asf(xfull[j] << 16); xv[2 * j + 1] = asf(xfull[j] & 0xffff0000u); }
    float sm = 0.f;
#pragma unroll
    for (int j = 0; j < 8; ++j) sm += xv[j];
    const float mean = wave_sum(sm) * (1.f / 512.f);
    float sq = 0.f;
#pragma unroll
    for (int j = 0; j < 8; ++j) sq += (xv[j] - mean) * (xv[j] - mean);
    const float mb = rbf(mean);
    const float rsd = __builtin_amdgcn_rcpf(rbf(__builtin_amdgcn_sqrtf(rbf(rbf(wave_sum(sq) * (1.f / 512.f)) + EPS))));
#pragma unroll
    for (int j = 0; j < 4; ++j) {
      const u32 c = pk_bf16(asf(xa0[j] << 16) - mb, asf(xa0[j] & 0xffff0000u) - mb);
      ya0[j] = pk_bf16(asf(c << 16) * rsd, asf(c & 0xffff0000u) * rsd);
    }
  }
  u32x4 wqt[8];
  tr8<4>(wqr, wqt);
  u32x4 ones = {0x3f803f80u, 0x3f803f80u, 0x3f803f80u, 0x3f803f80u};
  PIN(ones);
  auto none = []() {};
  const f32x4 zero4 = {0.f, 0.f, 0.f, 0.f};

  auto s1_core = [&](u32x4 a, int dst, bool on) {
    f32x4 d[8];
#pragma unroll
    for (int c = 0; c < 8; ++c) d[c] = mfma(a, wqt[c], zero4);
#pragma unroll
    for (int c = 0; c < 8; ++c) PIN(d[c]);
    float keep[4], send[4];
#pragma unroll
    for (int j = 0; j < 4; ++j) send[j] = bit3 ? d[j][1] : d[4 + j][0];
#pragma unroll
    for (int j = 0; j < 4; ++j) keep[j] = bit3 ? d[4 + j][1] : d[j][0];
    SCHED();
    u32x4 o;
#pragma unroll
    for (int j = 0; j < 4; ++j) { float t = keep[j] + dpp<ROR8>(send[j]); PIN(t); o[j] = tag8(t, t8v); }
    if (on) stsc1(rs, dst, o);
  };
  if (tid < 4) ((u32*)(s_x1 + 512))[tid] = 0x3f803f80u;
  s1_core(bit3 ? ones : ya0, (kg == 0 ? QP_OFF : CS_OFF) + ((b * 8 + gq) * 8 + s) * 2048 + w * 256 + i16 * 16, (kg & 1) == 0);
  STAMP(1);
  __builtin_amdgcn_sched_barrier(0);
  if (LBAR & 2) asm volatile("s_barrier" ::: "memory");
  __builtin_amdgcn_sched_barrier(0);
  load_v(0, VSPLIT);
  __builtin_amdgcn_sched_barrier(0);
  if (LBAR & 4) asm volatile("s_barrier" ::: "memory");
  __builtin_amdgcn_sched_barrier(0);
  const int rot = ROT * b;
  auto slot = [&](int c) { return (c & ~7) | ((c - rot) & 7); };
  auto load_wo = [&](int r0, int r1) {
    const uint16_t* p = wo + (size_t)(go * 512 + (((l >> 3) & 1) * 4 + kg) * 64 + w) * 512 + s * 64 + (l & 7) * 8;
#pragma unroll
    for (int r = r0; r < r1; ++r) wor[r] = *(const u32x4*)(p + ((r + rot) & 7) * 8 * 512);
  };
  auto load_w1 = [&](int r0, int r1) {
    const uint16_t* p = w1 + (size_t)(w * 64 + ((l >> 3) & 1) * 32 + kg * 8) * FF + f0 + (l & 7) * 4;
#pragma unroll
    for (int r = r0; r < r1; ++r) w1r[r] = *(const u32x2*)(p + ((r + rot) & 7) * FF);
  };
  auto load_w2 = [&](int r0, int r1) {
    const uint16_t* p2 = w2 + (size_t)(f0 + kg * 8) * 512 + w * 64 + i16 * 4;
#pragma unroll
    for (int r = r0; r < r1; ++r) w2r[r] = *(const u32x2*)(p2 + ((r + rot) & 7) * 512);
  };

  const int xo = X2_OFF + bg * 2048 + w * 256 + c8 * 4;
  u32x4 vt[8], wot[8], w1t[4], w2t[4];
  float csq = 0.f, cs1 = 0.f;
  auto red8 = [&](const float (&v)[8]) {
    const float u0 = rs32(v[0], v[4]), u1 = rs32(v[1], v[5]), u2 = rs32(v[2], v[6]), u3 = rs32(v[3], v[7]);
    return rs8(rs16(u0, u2), rs16(u1, u3), bit3);
  };

#pragma unroll
  for (int layer = 0; layer < 2; ++layer) {
    {
      const int qo = (bg * 8 + (l >> 3)) * 2048 + w * 256 + (l & 7) * 16;
      int od = OP_OFF + ((layer * 32 + bg) * 8 + s) * 2048 + w * 256 + ((slot(l) >> 2) & 1) * 128 + (l >> 3) * 16 + (slot(l) & 3) * 4;
      PIN(od);
      float a[8], mean = 0.f, rstd = 1.f;
      DLY(layer * 4);
      if (layer == 0) {
        u32x4 v[4];
        const int off[4] = {QP_OFF + qo, QP_OFF + qo + 128, CS_OFF + qo, CS_OFF + qo + 128};
        gather<4>(rs, off, t8, v, [&]() { load_v(VSPLIT, 8); load_wo(0, WO_A); });
        STAMP(2);
        float c[8];
#pragma unroll
        for (int j = 0; j < 4; ++j) { a[j] = asf(v[0][j]); a[4 + j] = asf(v[1][j]); c[j] = asf(v[2][j]); c[4 + j] = asf(v[3][j]); }
        csq = red8(c);
      } else {
        u32x4 v[3];
        const int off[3] = {ST_OFF + (bg * 8 + (l >> 3)) * 128 + (l & 7) * 16, QP_OFF + 256 * 2048 + qo, QP_OFF + 256 * 2048 + qo + 128};
        gather<3>(rs, off, t8, v, [&]() { xres = __builtin_amdgcn_raw_buffer_load_b32(rs, xo, 0, AUX_SC1); });
        STAMP(10);
        CST(0);
#pragma unroll
        for (int j = 0; j < 4; ++j) { a[j] = asf(v[1][j]); a[4 + j] = asf(v[2][j]); }
        float u = rs32(asf(v[0][0]) + asf(v[0][1]), asf(v[0][2]) + asf(v[0][3]));
        u = rs16(u, u);
        u += dpp<ROR8>(u);
        auto r = __builtin_amdgcn_permlane32_swap(asu(u), asu(u), false, false);
        mean = asf(r[0]) * (1.f / 512.f);
        rstd = __builtin_amdgcn_rsqf(fmx(asf(r[1]) * (1.f / 512.f) - mean * mean, 0.f) + EPS);
      }
      const float qv = layer == 0 ? red8(a) : (red8(a) - mean * csq) * rstd;
      s_q[w * 64 + c8] = (uint16_t)(pk_bf16(qv, qv) & 0xffffu);
      CST(1);
      wg_barrier();
      CST(2);
      const u32x4 qf = *(const u32x4*)(s_q + (i16 >> 2) * 128 + kg * 32 + (i16 & 3) * 8);
      f32x4 d[8];
#pragma unroll
      for (int m = 0; m < 8; ++m) d[m] = mfma(kr[m], qf, zero4);
      SCHED();
      float sv[8];
#pragma unroll
      for (int m = 0; m < 8; ++m) { sv[m] = d[m][0] + dpp<0x55>(d[m][1]); PIN(sv[m]); }
#pragma unroll
      for (int m = 0; m < 8; ++m) { sv[m] += dpp<0xAA>(d[m][2]); PIN(sv[m]); }
#pragma unroll
      for (int m = 0; m < 8; ++m) { sv[m] += dpp<0xFF>(d[m][3]); PIN(sv[m]); }
      CST(3);
      if (layer == 0) {
#pragma unroll
        for (int j = 0; j < 4; ++j) {
          const u32 c = pk_bf16(sv[2 * j], sv[2 * j + 1]);
          const f32x2 t = f32x2{asf(c << 16), asf(c & 0xffff0000u)} * 0.08838834764831845f;
          const u32 e = pk_bf16(t[0], t[1]);
          sv[2 * j] = asf(e << 16); sv[2 * j + 1] = asf(e & 0xffff0000u);
        }
      }
      const float sc = (layer == 0 ? 1.f : 0.08838834764831845f) * 1.4426950408889634f;
      float m = fmx(fmx(fmx(fmx(sv[0], sv[1]), sv[2]), fmx(fmx(sv[3], sv[4]), sv[5])), fmx(sv[6], sv[7]));
      m = mx32(mx16(m)) * sc;
      u32x4 pa;
#pragma unroll
      for (int j = 0; j < 4; ++j) {
        const f32x2 e = f32x2{sv[2 * j], sv[2 * j + 1]} * sc - m;
        pa[j] = pk_bf16(__builtin_amdgcn_exp2f(e[0]), __builtin_amdgcn_exp2f(e[1]));
      }
      CST(4);
      if (layer == 0) { SCHED(); load_wo(WO_A, WO_B); load_w1(0, W_E); load_w2(0, W_E); SCHED(); tr8<4>(vr, vt); }
      f32x4 dl = mfma(pa, ones, zero4);
      SCHED();
#pragma unroll
      for (int c2 = 0; c2 < 8; ++c2) d[c2] = mfma(pa, vt[c2], zero4);
      PIN(dl);
#pragma unroll
      for (int c2 = 0; c2 < 8; ++c2) PIN(d[c2]);
      if (i16 == 4 * kg) { s_ml[kg * 16 + w] = m; s_ml[kg * 16 + 8 + w] = dl[0]; }
#pragma unroll
      for (int c2 = 0; c2 < 8; ++c2) s_ow[(w * 8 + c2) * 64 + l] = d[c2][0];
      CST(5);
      wg_barrier();
      CST(6);
      const f32x4 m0 = *(const f32x4*)(s_ml + kg * 16), m1 = *(const f32x4*)(s_ml + kg * 16 + 4);
      const f32x4 l0 = *(const f32x4*)(s_ml + kg * 16 + 8), l1 = *(const f32x4*)(s_ml + kg * 16 + 12);
      float ov[8];
#pragma unroll
      for (int u = 0; u < 8; ++u) ov[u] = s_ow[(u * 8 + w) * 64 + l];
      const float M = fmx(fmx(fmx(fmx(m0[0], m0[1]), m0[2]), fmx(fmx(m0[3], m1[0]), m1[1])), fmx(m1[2], m1[3]));
      const f32x2 mp[4] = {lo2(m0), hi2(m0), lo2(m1), hi2(m1)}, lp[4] = {lo2(l0), hi2(l0), lo2(l1), hi2(l1)};
      f32x2 L2, o2;
#pragma unroll
      for (int u = 0; u < 4; ++u) {
        const f32x2 e = mp[u] - M;
        const f32x2 c = {__builtin_amdgcn_exp2f(e[0]), __builtin_amdgcn_exp2f(e[1])};
        const f32x2 ou = {ov[2 * u], ov[2 * u + 1]};
        L2 = u ? c * lp[u] + L2 : c * lp[u];
        o2 = u ? c * ou + o2 : c * ou;
      }
      PIN(L2); PIN(o2);
      const float L = L2[0] + L2[1];
      const float on = (o2[0] + o2[1]) * __builtin_amdgcn_rcpf(L);
      const float lw = M + __builtin_amdgcn_logf(L);
      __builtin_amdgcn_raw_buffer_store_b32(__builtin_amdgcn_perm(pk_bf16(on, on), asu(lw), 0x0706000cu | ((slot(l) & 3) << 8)) | t8, rs, od, 0, AUX_SC1);
      STAMP(3 + layer * 8);
      CST(7);
    }
    {
      if (layer == 0) { SCHED(); load_wo(WO_B, 8); SCHED(); }
      const int oo = OP_OFF + ((layer * 32 + b * 8 + go) * 8 + (l & 7)) * 2048 + w * 256 + (((l >> 3) & 1) * 4 + kg) * 16;
      const int off[2] = {oo, oo + 128};
      int wd = WP_OFF + (layer * 4 + b) * 16384 + g * 2048 + s * 256 + (l & 1) * 128 + (i16 >> 1) * 16 + (l >> 5) * 8 + (kg & 1) * 4;
      PIN(wd);
      u32x4 v[2];
      DLY(1 + layer * 4);
      if (layer == 0) gather<2, false>(rs, off, t8, v, [&]() {
        if (W1POS == 0) { load_w1(W_E, 8); SCHED(); }
        tr8<4>(wor, wot);
#pragma unroll
        for (int c = 0; c < 8; ++c) PIN(wot[c]);
      }); else gather<2, false>(rs, off, t8, v, [&]() {
        if (__builtin_expect(!__all(((xres ^ t16) & 0xffffu) == 0), 0)) {
          for (u32 spins = 0; spins < SPIN_CAP; ++spins) {
            asm volatile("" ::: "memory");
            xres = __builtin_amdgcn_raw_buffer_load_b32(rs, xo, 0, AUX_SC1);
            if (__all(((xres ^ t16) & 0xffffu) == 0)) break;
          }
        }
        xres &= 0xffff0000u;
        PIN(xres);
      });
      STAMP(4 + layer * 8);
      CST(8);
      if (layer == 0 && W1POS == 2) { SCHED(); load_w1(W_E, 8); SCHED(); }
      const float lw = asf(__builtin_amdgcn_perm(__builtin_amdgcn_perm(v[0][3], v[0][2], 0x05010000u), __builtin_amdgcn_perm(v[0][1], v[0][0], 0x00000501u), 0x07060100u));
      float M = fmx(lw, dpp<QX1>(lw)); M = fmx(M, dpp<QX2>(M)); M = fmx(M, dpp<HMIR>(M));
      float cw = __builtin_amdgcn_exp2f(lw - M);
      float T = cw + dpp<QX1>(cw); T += dpp<QX2>(T); T += dpp<HMIR>(T);
      cw *= __builtin_amdgcn_rcpf(T);
      u32x4 a;
#pragma unroll
      for (int j = 0; j < 4; ++j)
        a[j] = pk_bf16(asf(v[j >> 1][(j & 1) * 2] & 0xffff0000u) * cw, asf(v[j >> 1][(j & 1) * 2 + 1] & 0xffff0000u) * cw);
      f32x4 d[8];
#pragma unroll
      for (int c = 0; c < 8; ++c) d[c] = mfma(a, wot[c], zero4);
      f32x4 z[2];
#pragma unroll
      for (int c = 0; c < 8; ++c) { f32x2 t = lo2(d[c]) + hi2(d[c]); PIN(t); float r = t[0] + t[1]; PIN(r); z[c >> 2][c & 3] = r; }
      if (((l >> 3) & 1) == (l >> 5)) {
        *(f32x4*)(s_wp + (w * 4 + kg) * 64 + (l & 7) * 8) = z[0];
        *(f32x4*)(s_wp + (w * 4 + kg) * 64 + (l & 7) * 8 + 4) = z[1];
      }
      if (layer == 0 && W1POS == 3) { SCHED(); load_w1(W_E, 8); SCHED(); }
      CST(9);
      wg_barrier();
      CST(10);
      if (w == 0) {
        f32x4 p[8];
#pragma unroll
        for (int k = 0; k < 8; ++k) p[k] = *(const f32x4*)(s_wp + (kg * 8 + k) * 64 + i16 * 4);
        const f32x4 acc = ((p[0] + p[1]) + (p[2] + p[3])) + ((p[4] + p[5]) + (p[6] + p[7]));
        const float t = rs16(rs32(acc[0], acc[2]), rs32(acc[1], acc[3]));
        __builtin_amdgcn_raw_buffer_store_b32(tag8(t, t8v), rs, wd, 0, AUX_SC1);
      }
      STAMP(5 + layer * 8);
      CST(11);
    }
    {
      if (layer == 0 && W1POS == 1) { SCHED(); load_w1(W_E, 8); SCHED(); }
      if (layer == 0 && W2POS == 1) { SCHED(); load_w2(W_E, 8); SCHED(); }
      const int wo_ = WP_OFF + (layer * 4 + b) * 16384 + (l >> 3) * 2048 + w * 256 + (l & 7) * 16;
      const int off[2] = {wo_, wo_ + 128};
      int md = MP_OFF + (((layer * 4 + b) * 8 + w) * 56 + g * 7 + s) * 256 + l * 16;
      PIN(md);
      u32x4 v[2];
      DLY(2 + layer * 4);
      if (layer == 0) gather<2>(rs, off, t8, v, [&]() {
        if (W2POS == 0) load_w2(W_E, 8);
        SCHED();
        tr8<2>(w1r, w1t);
#pragma unroll
        for (int c = 0; c < 4; ++c) PIN(w1t[c]);
      }); else gather<2>(rs, off, t8, v, none);
      STAMP(6 + layer * 8);
      CST(12);
      float y[8];
#pragma unroll
      for (int j = 0; j < 8; ++j) y[j] = asf(v[j >> 2][j & 3]);
      const float x1 = rbf(red8(y)) + asf(xres);
      s_x1[w * 64 + slot(c8)] = (uint16_t)(pk_bf16(x1, x1) & 0xffffu);
      asm volatile("" ::: "memory");
      const int xf = w * 64 + (i16 & 1) * 32 + kg * 8;
      const u32x4 am = *(const u32x4*)(s_x1 + (bit3 ? 512 : xf));
      const u32x4 bx = *(const u32x4*)(s_x1 + xf);
      f32x4 d[4];
#pragma unroll
      for (int c = 0; c < 4; ++c) d[c] = mfma(am, w1t[c], zero4);
      f32x4 gr = mfma(am, bx, zero4);
#pragma unroll
      for (int c = 0; c < 4; ++c) PIN(d[c]);
      if (l < 16 || (layer == 0 && kg == 2)) {
        const f32x4 hv = bit3 ? f32x4{d[0][1], d[1][1], d[2][1], d[3][1]} : f32x4{d[0][0], d[1][0], d[2][0], d[3][0]};
        *(f32x4*)(s_hp + (l >> 5) * 512 + (w * 2 + ((l >> 3) & 1)) * 32 + (l & 7) * 4) = hv;
      }
      if ((l & 30) == 0) s_st[(l >> 5) * 16 + w * 2 + (l & 1)] = (l & 1) ? gr[1] : gr[0];
      if (layer == 0) tr8<2>(w2r, w2t);
      wg_barrier();
      if (mlp_on) {
        float st = s_st[l & 31];
        float hp8[8];
#pragma unroll
        for (int p = 0; p < 8; ++p) hp8[p] = s_hp[((l >> 5) * 8 + p) * 32 + (l & 31)];
        if (layer == 0) {
          float ac = 0.f;
#pragma unroll
          for (int p = 0; p < 8; ++p) ac += s_hp[512 + ((l >> 5) * 8 + p) * 32 + (l & 31)];
          cs1 = rs32(ac, ac);
        }
        st += dpp<ROR8>(st); st += dpp<ROR4>(st); st += dpp<ROR2>(st); st += dpp<ROR1>(st);
        float acc = sum8(hp8);
        acc = rs32(acc, acc);
        auto r = __builtin_amdgcn_permlane16_swap(asu(st), asu(st), false, false);
        const float mu = asf(r[1]) * (1.f / 512.f);
        const float var = fmx(asf(r[0]) * (1.f / 512.f) - mu * mu, 0.f);
        const float r12 = __builtin_amdgcn_rsqf(var + EPS * (var + EPS));
        const float hp = (acc - mu * cs1) * r12 + b1v;
        const float u = hp * (-2.302208198144325f - 0.1029432395800235f * (hp * hp));
        const float hv = hp * __builtin_amdgcn_rcpf(1.f + __builtin_amdgcn_exp2f(u));
        s_a[w * 64 + slot(l)] = (uint16_t)(pk_bf16(hv, hv) & 0xffffu);
        asm volatile("" ::: "memory");
        const u32x4 ha = *(const u32x4*)(s_a + w * 64 + kg * 8);
        f32x4 e[4];
#pragma unroll
        for (int c = 0; c < 4; ++c) e[c] = mfma(ha, w2t[c], zero4);
#pragma unroll
        for (int c = 0; c < 4; ++c) PIN(e[c]);
        const u32x4 o = {tag8(e[0][0], t8v), tag8(e[1][0], t8v), tag8(e[2][0], t8v), tag8(e[3][0], t8v)};
        if (l < 16) stsc1(rs, md, o);
      }
      STAMP(7 + layer * 8);
      CST(13);
    }
    if (layer == 0) {
      float xr = asf((u32)s_x1[s * 64 + slot(l)] << 16);
      PIN(xr);
      const int mo = MP_OFF + ((b * 8 + s) * 56 + w * 7 + kg) * 256 + i16 * 16;
      const int off[2] = {mo, kg < 3 ? mo + 1024 : mo};
      float two = kg < 3 ? 1.f : 0.f;
      int qd = QP_OFF + ((32 + b * 8 + gq) * 8 + s) * 2048 + w * 256 + i16 * 16;
      int sd = ST_OFF + ((b * 8 + gq) * 8 + s) * 128 + (l & 7) * 16 + (l >> 5) * 8;
      int xpi = w * 64 + i16 * 4 + (l >> 5) * 2 + (kg & 1), sai = w * 64 + (i16 & 1) * 32 + kg * 8;
      float b2p = b2a;
      PIN(two); PIN(qd); PIN(sd); PIN(xpi); PIN(sai); PIN(b2p);
      u32x4 v[2];
      DLY(3);
      gather<2>(rs, off, t8, v, none);
      STAMP(8);
      float y[8];
#pragma unroll
      for (int j = 0; j < 4; ++j) y[j] = asf(v[0][j]) + two * asf(v[1][j]);
      s_xp[xpi] = rs16(rs32(y[0], y[2]), rs32(y[1], y[3]));
      wg_barrier();
#pragma unroll
      for (int u = 0; u < 8; ++u) y[u] = s_xp[u * 64 + l];
      const float x2f = rbf(rbf(sum8(y)) + b2p) + xr;
      const u32 x2 = pk_bf16(x2f, x2f);
      s_a[w * 64 + l] = (uint16_t)(x2 & 0xffffu);
      asm volatile("" ::: "memory");
      const u32x4 a = *(const u32x4*)(s_a + sai);
      if (w == 6) {
        const float xv = asf(x2 & 0xffff0000u);
        float u = rs32(xv, xv * xv);
        u = rs16(u, u);
        u += dpp<ROR8>(u); u += dpp<ROR4>(u); u += dpp<ROR2>(u); u += dpp<ROR1>(u);
        const float hi = asf(asu(u) & 0xffffff00u);
        const u32x2 o = {asu(hi) | t8, tag8(u - hi, t8v)};
        if ((l & 24) == 0) __builtin_amdgcn_raw_buffer_store_b64(o, rs, sd, 0, AUX_SC1);
      }
      s1_core(a, qd, l < 16);
      if (w == 7) __builtin_amdgcn_raw_buffer_store_b32((x2 & 0xffff0000u) | t16, rs, X2_OFF + (b * 8 + gq) * 2048 + s * 256 + l * 4, 0, AUX_SC1);
      STAMP(9);
    } else if (w == 0) {
      const int oc = s * 64 + g * 8 + e8;
      const float xr = asf((u32)s_x1[slot(oc)] << 16);
      const int mo = MP_OFF + (((4 + b) * 8 + s) * 56 + (l < 56 ? l : 0)) * 256 + g * 32;
      const int off[2] = {mo, mo + 16};
      u32x4 v[2];
      DLY(7);
      gather<2>(rs, off, t8, v, none);
      STAMP(16);
      float y[8];
#pragma unroll
      for (int j = 0; j < 8; ++j) y[j] = l < 56 ? asf(v[j >> 2][j & 3]) : 0.f;
      float t = red8(y);
      t += dpp<QX1>(t); t += dpp<QX2>(t); t += dpp<HMIR>(t);
      const float r = rbf(rbf(t) + b2f) + xr;
      if ((l & 7) == 0) out[b * 512 + oc] = (uint16_t)(pk_bf16(r, r) & 0xffffu);
      if (l == 0 && g == 0 && s == 0) __builtin_amdgcn_raw_buffer_store_b32(tn, rs, EP_OFF + b * 256, 0, AUX_SC1);
      STAMP(17);
    }
  }
}

extern "C" void kernel_launch(void* const* d_in, const int* in_sizes, int n_in, void* d_out, int out_size, void* d_ws,
                              size_t ws_size, hipStream_t stream) {
  (void)in_sizes; (void)n_in; (void)out_size; (void)ws_size;
  fused_decode<<<256, 512, 0, stream>>>((const uint16_t*)d_in[0], (const uint16_t*)d_in[1], (const uint16_t*)d_in[2],
                                        (const uint16_t*)d_in[3], (const uint16_t*)d_in[4], (const uint16_t*)d_in[6],
                                        (const uint16_t*)d_in[7], (const uint16_t*)d_in[8], (const uint16_t*)d_in[9],
                                        (uint16_t*)d_out, (char*)d_ws TUNE_ARG);
}
```

```cpp
#include <hip/hip_runtime.h>
#include <hip/hip_bf16.h>
#include <stdint.h>

#define DIAG 0

typedef unsigned int u32;
typedef __attribute__((ext_vector_type(4))) unsigned int u32x4;
typedef __attribute__((ext_vector_type(2))) unsigned int u32x2;
typedef __attribute__((ext_vector_type(4))) float f32x4;
typedef __attribute__((ext_vector_type(2))) float f32x2;
typedef __attribute__((ext_vector_type(8))) __bf16 bf16x8;
typedef __attribute__((ext_vector_type(2))) __bf16 bf16x2;

namespace {
constexpr int SK = 8192, WIN = 2048, FF = 1792;
constexpr float EPS = 1e-5f;
constexpr u32 SPIN_CAP = 1u << 17;
constexpr int EP_OFF = 0;
constexpr int QP_OFF = 4096;
constexpr int CS_OFF = QP_OFF + 2 * 256 * 2048;
constexpr int X2_OFF = CS_OFF + 256 * 2048;
constexpr int OP_OFF = X2_OFF + 32 * 2048;
constexpr int ST_OFF = OP_OFF + 2 * 256 * 2048;
constexpr int WP_OFF = ST_OFF + 256 * 128;
constexpr int MP_OFF = WP_OFF + 2 * 4 * 16384;
constexpr int WS_END = MP_OFF + 2 * 4 * 8 * 56 * 256;
constexpr int DG_OFF = WS_END;
constexpr int AUX_SC1 = 16;

__device__ __forceinline__ float asf(u32 x) { return __builtin_bit_cast(float, x); }
__device__ __forceinline__ u32 asu(float x) { return __builtin_bit_cast(u32, x); }
__device__ __forceinline__ u32 pk_bf16(float lo, float hi) {
  f32x2 v = {lo, hi};
  return __builtin_bit_cast(u32, __builtin_convertvector(v, bf16x2));
}
__device__ __forceinline__ float rbf(float x) {
  return asf(pk_bf16(x, x) & 0xffff0000u);
}
__device__ __forceinline__ float fmx(float a, float b) { return __builtin_elementwise_maximum(a, b); }
#define PIN(x) asm volatile("" : "+v"(x))
#define SCHED() __builtin_amdgcn_sched_barrier(0)
template <int CTRL> __device__ __forceinline__ float dpp(float x) {
  return asf(__builtin_amdgcn_update_dpp(0, asu(x), CTRL, 0xf, 0xf, true));
}
constexpr int ROR8 = 0x128, ROR4 = 0x124, ROR2 = 0x122, ROR1 = 0x121, QX1 = 0xB1, QX2 = 0x4E, HMIR = 0x141;
__device__ __forceinline__ float rs32(float a, float b) {
  auto r = __builtin_amdgcn_permlane32_swap(asu(a), asu(b), false, false);
  return asf(r[0]) + asf(r[1]);
}
__device__ __forceinline__ float rs16(float a, float b) {
  auto r = __builtin_amdgcn_permlane16_swap(asu(a), asu(b), false, false);
  return asf(r[0]) + asf(r[1]);
}
__device__ __forceinline__ float rs8(float a, float b, bool bit3) {
  return (bit3 ? b : a) + dpp<ROR8>(bit3 ? a : b);
}
__device__ __forceinline__ float mx32(float a) {
  auto r = __builtin_amdgcn_permlane32_swap(asu(a), asu(a), false, false);
  return fmx(asf(r[0]), asf(r[1]));
}
__device__ __forceinline__ float mx16(float a) {
  auto r = __builtin_amdgcn_permlane16_swap(asu(a), asu(a), false, false);
  return fmx(asf(r[0]), asf(r[1]));
}
__device__ __forceinline__ float wave_sum(float a) {
  a += dpp<ROR8>(a); a += dpp<ROR4>(a); a += dpp<ROR2>(a); a += dpp<ROR1>(a);
  a = rs16(a, a); return rs32(a, a);
}
__device__ __forceinline__ f32x4 mfma(u32x4 a, u32x4 b, f32x4 c) {
  return __builtin_amdgcn_mfma_f32_16x16x32_bf16(__builtin_bit_cast(bf16x8, a), __builtin_bit_cast(bf16x8, b), c, 0, 0, 0);
}
template <int NW, typename V> __device__ __forceinline__ void tr8(const V (&r)[8], u32x4 (&t)[2 * NW]) {
#pragma unroll
  for (int c = 0; c < NW; ++c)
#pragma unroll
    for (int j = 0; j < 4; ++j) {
      t[2 * c][j] = __builtin_amdgcn_perm(r[2 * j + 1][c], r[2 * j][c], 0x05040100u);
      t[2 * c + 1][j] = __builtin_amdgcn_perm(r[2 * j + 1][c], r[2 * j][c], 0x07060302u);
    }
}
__device__ __forceinline__ u32x4 ldsc1(__amdgpu_buffer_rsrc_t rs, int off) {
  return __builtin_amdgcn_raw_buffer_load_b128(rs, off, 0, AUX_SC1);
}
__device__ __forceinline__ void stsc1(__amdgpu_buffer_rsrc_t rs, int off, u32x4 v) {
  __builtin_amdgcn_raw_buffer_store_b128(v, rs, off, 0, AUX_SC1);
}
__device__ __forceinline__ u32 tag8(float v, u32 t8) { return (asu(v) & 0xffffff00u) | t8; }
__device__ __forceinline__ u32 or4(u32x4 v) { return (v[0] | v[1]) | (v[2] | v[3]); }
__device__ __forceinline__ void wg_barrier() {
  asm volatile("s_waitcnt lgkmcnt(0)\n\ts_barrier" ::: "memory");
}
template <int N, bool CLEAN> __device__ __forceinline__ bool untag(u32x4 (&v)[N], u32 tg) {
  u32 bad = 0;
#pragma unroll
  for (int k = 0; k < N; ++k) {
    if (CLEAN) { v[k] ^= tg; bad |= or4(v[k]); }
    else bad |= __builtin_amdgcn_bitop3_b32(v[k][0], v[k][1], tg, 0x7e) | __builtin_amdgcn_bitop3_b32(v[k][2], v[k][3], tg, 0x7e);
  }
  return __all((bad & 0xffu) == 0);
}
template <int N, bool CLEAN = true, typename F>
__device__ __forceinline__ void gather(__amdgpu_buffer_rsrc_t rs, const int (&off)[N], u32 tg, u32x4 (&v)[N], F&& behind) {
#pragma unroll
  for (int k = 0; k < N; ++k) v[k] = ldsc1(rs, off[k]);
  SCHED();
  behind();
  SCHED();
  if (__builtin_expect(!untag<N, CLEAN>(v, tg), 0)) {
    for (u32 spins = 0; spins < SPIN_CAP; ++spins) {
      asm volatile("" ::: "memory");
#pragma unroll
      for (int k = 0; k < N; ++k) v[k] = ldsc1(rs, off[k]);
      if (untag<N, CLEAN>(v, tg)) break;
    }
  }
}
__device__ __forceinline__ f32x2 lo2(f32x4 v) { return __builtin_shufflevector(v, v, 0, 1); }
__device__ __forceinline__ f32x2 hi2(f32x4 v) { return __builtin_shufflevector(v, v, 2, 3); }
__device__ __forceinline__ float sum8(const float (&y)[8]) {
  f32x2 t = (f32x2{y[0], y[1]} + f32x2{y[2], y[3]}) + (f32x2{y[4], y[5]} + f32x2{y[6], y[7]});
  PIN(t);
  return t[0] + t[1];
}
}

#define STAMP(i) do {} while (0)
#define CST(i) do {} while (0)
#define KDELAY {0, 18, 18, 18, 4, 18, 18, 18}
constexpr int kDelay[8] = KDELAY;
#define DLY(i) do { if (kDelay[i] > 0) asm volatile("s_sleep %0" :: "i"(kDelay[i])); } while (0)
#define TUNE_PARAM
#define TUNE_ARG
#define QSHIFT 1
#define VSPLIT 2
#define LBAR 4
#define WO_A 0
#define WO_B 4
#define ROT 2
#define W_E 0
#define W2POS 1
#define W1POS 2
#define WQPOS 1
#define KLATE 1
#define PRIO 1
#define OSHIFT 1

__global__ void __launch_bounds__(512, 2)
fused_decode(const uint16_t* __restrict__ x, const uint16_t* __restrict__ kc, const uint16_t* __restrict__ vc,
             const uint16_t* __restrict__ wq, const uint16_t* __restrict__ wo, const uint16_t* __restrict__ w1,
             const uint16_t* __restrict__ b1, const uint16_t* __restrict__ w2, const uint16_t* __restrict__ b2,
             uint16_t* __restrict__ out, char* ws TUNE_PARAM) {
  __shared__ __attribute__((aligned(16))) float s_ow[64 * 64];
  __shared__ __attribute__((aligned(16))) float s_ml[4 * 16];
  __shared__ __attribute__((aligned(16))) uint16_t s_q[512];
  __shared__ __attribute__((aligned(16))) uint16_t s_x1[512 + 8];
  __shared__ __attribute__((aligned(16))) float s_hp[2 * 16 * 32];
  __shared__ __attribute__((aligned(16))) float s_st[32];
  __shared__ __attribute__((aligned(16))) uint16_t s_a[8 * 64];
  __shared__ __attribute__((aligned(16))) float s_xp[8 * 64];
  __shared__ __attribute__((aligned(16))) float s_wp[32 * 64];

  const int tid = threadIdx.x, bid = blockIdx.x;
  const int w = __builtin_amdgcn_readfirstlane(tid >> 6);
  const int l = tid & 63, i16 = l & 15, kg = l >> 4;
  const int g = bid & 7, s = (bid >> 3) & 7, b = bid >> 6;
  const int bg = b * 8 + g;
  const bool bit3 = (l & 8) != 0;
  const bool mlp_on = s < 7;
  const int f0 = g * 224 + (mlp_on ? s : 6) * 32;
  const __amdgpu_buffer_rsrc_t rs = __builtin_amdgcn_make_buffer_rsrc((void*)ws, 0, WS_END, 0x00020000);
  STAMP(0);
  const int gq = (g + QSHIFT) & 7, go = (g + OSHIFT) & 7;

  const u32 ep = __builtin_amdgcn_raw_buffer_load_b32(rs, EP_OFF + b * 256, 0, AUX_SC1);
  const bool lead = (w >> 1) == b;
  const int e8 = (l >> 5) * 4 + ((l >> 4) & 1) * 2 + ((l >> 3) & 1);
  const int c8 = (l & 7) * 8 + e8;
  u32x4 wqr[8], kr[8], vr[8], wor[8];
  u32x2 w1r[8], w2r[8];
  auto load_wq = [&]() {
    const uint16_t* p = wq + (size_t)(s * 64 + ((l >> 3) & 1) * 32 + kg * 8) * 4096 + gq * 512 + w * 64 + (l & 7) * 8;
#pragma unroll
    for (int r = 0; r < 8; ++r) wqr[r] = *(const u32x4*)(p + r * 4096);
  };
  u32x4 xfull[4], xa0;
  u32 ep2[2];
  if (lead) {
#pragma unroll
    for (int j = 0; j < 4; ++j) xfull[j] = *(const u32x4*)(x + kg * 512 + j * 128 + i16 * 8);
    xa0 = *(const u32x4*)(x + ((i16 >> 1) & 3) * 512 + s * 64 + (i16 & 1) * 32 + kg * 8);
#pragma unroll
    for (int j = 0; j < 2; ++j) ep2[j] = __builtin_amdgcn_raw_buffer_load_b32(rs, EP_OFF + ((kg & 1) * 2 + j) * 256, 0, AUX_SC1);
    load_wq();
  }
  __builtin_amdgcn_sched_barrier(0);
  if (!(LBAR & 8)) asm volatile("s_barrier" ::: "memory");
  __builtin_amdgcn_sched_barrier(0);
  const size_t kvbase = ((size_t)bg * SK + (SK - WIN) + s * 256 + w * 32) * 128;
  auto load_k = [&]() {
    const uint16_t* p = kc + kvbase + (i16 >> 2) * 8 * 128 + kg * 32 + (i16 & 3) * 8;
#pragma unroll
    for (int m = 0; m < 8; ++m) kr[m] = __builtin_nontemporal_load((const u32x4*)(p + m * 128));
  };
  if (!KLATE) load_k();
  auto load_v = [&](int r0, int r1) {
    const uint16_t* p = vc + kvbase + kg * 8 * 128 + i16 * 8;
#pragma unroll
    for (int r = r0; r < r1; ++r) vr[r] = __builtin_nontemporal_load((const u32x4*)(p + r * 128));
  };
  __builtin_amdgcn_sched_barrier(0);
  if (LBAR & 1) asm volatile("s_barrier" ::: "memory");
  const float b1v = asf((u32)b1[f0 + (l & 31)] << 16);
  const float b2a = asf((u32)b2[s * 64 + l] << 16);
  const float b2f = asf((u32)b2[s * 64 + g * 8 + e8] << 16);
  u32 xres = (u32)x[b * 512 + w * 64 + c8] << 16;
  __builtin_amdgcn_sched_barrier(0);

  u32x4 wqt[8];
  u32x4 ones = {0x3f803f80u, 0x3f803f80u, 0x3f803f80u, 0x3f803f80u};
  PIN(ones);
  auto none = []() {};
  const f32x4 zero4 = {0.f, 0.f, 0.f, 0.f};

  auto s1_out = [&](const f32x4 (&d)[8], int r, u32 tg, int dst, bool on) {
    float keep[4], send[4];
#pragma unroll
    for (int j = 0; j < 4; ++j) send[j] = bit3 ? d[j][r + 1] : d[4 + j][r];
#pragma unroll
    for (int j = 0; j < 4; ++j) keep[j] = bit3 ? d[4 + j][r + 1] : d[j][r];
    SCHED();
    u32x4 o;
#pragma unroll
    for (int j = 0; j < 4; ++j) { float t = keep[j] + dpp<ROR8>(send[j]); PIN(t); o[j] = tag8(t, tg); }
    if (on) stsc1(rs, dst, o);
  };
  auto s1_mm = [&](u32x4 a, f32x4 (&d)[8]) {
#pragma unroll
    for (int c = 0; c < 8; ++c) d[c] = mfma(a, wqt[c], zero4);
#pragma unroll
    for (int c = 0; c < 8; ++c) PIN(d[c]);
  };
  if (tid < 4) ((u32*)(s_x1 + 512))[tid] = 0x3f803f80u;
  if (lead) {
    if (PRIO) __builtin_amdgcn_s_setprio(3);
    PIN(ep2[0]); PIN(ep2[1]);
    float xv[32];
#pragma unroll
    for (int j = 0; j < 16; ++j) { xv[2 * j] = asf(xfull[j >> 2][j & 3] << 16); xv[2 * j + 1] = asf(xfull[j >> 2][j & 3] & 0xffff0000u); }
    auto row_sum = [&](float u) { u += dpp<ROR8>(u); u += dpp<ROR4>(u); u += dpp<ROR2>(u); u += dpp<ROR1>(u); return u; };
    f32x2 sm = {0.f, 0.f};
#pragma unroll
    for (int j = 0; j < 16; ++j) sm += f32x2{xv[2 * j], xv[2 * j + 1]};
    const float mean = row_sum(sm[0] + sm[1]) * (1.f / 512.f);
    f32x2 sq = {0.f, 0.f};
#pragma unroll
    for (int j = 0; j < 16; ++j) { const f32x2 c = f32x2{xv[2 * j], xv[2 * j + 1]} - mean; sq += c * c; }
    const float rk = __builtin_amdgcn_rcpf(rbf(__builtin_amdgcn_sqrtf(rbf(rbf(row_sum(sq[0] + sq[1]) * (1.f / 512.f)) + EPS))));
    const int src = ((i16 >> 1) & 3) * 64;
    const float mb = asf(__builtin_amdgcn_ds_bpermute(src, asu(rbf(mean))));
    const float rsd = asf(__builtin_amdgcn_ds_bpermute(src, asu(rk)));
    u32x4 ya0;
#pragma unroll
    for (int j = 0; j < 4; ++j) {
      const u32 c = pk_bf16(asf(xa0[j] << 16) - mb, asf(xa0[j] & 0xffff0000u) - mb);
      ya0[j] = pk_bf16(asf(c << 16) * rsd, asf(c & 0xffff0000u) * rsd);
    }
    tr8<4>(wqr, wqt);
    f32x4 d[8];
    s1_mm(bit3 ? ones : ya0, d);
    const int dst = (kg < 2 ? QP_OFF : CS_OFF) + (((kg & 1) * 16 + gq) * 8 + s) * 2048 + w * 256 + i16 * 16;
    s1_out(d, 0, (ep2[0] + 1u) & 0xffu, dst, true);
    s1_out(d, 2, (ep2[1] + 1u) & 0xffu, dst + 8 * 8 * 2048, true);
    if (PRIO) __builtin_amdgcn_s_setprio(0);
  }
  SCHED();
  if (KLATE) load_k();
  SCHED();
  STAMP(1);
  __builtin_amdgcn_sched_barrier(0);
  if (LBAR & 2) asm volatile("s_barrier" ::: "memory");
  __builtin_amdgcn_sched_barrier(0);
  load_v(0, VSPLIT);
  __builtin_amdgcn_sched_barrier(0);
  const u32 tn = __builtin_amdgcn_readfirstlane(ep) + 1u;
  const u32 t8 = tn & 0xffu, t16 = tn & 0xffffu;
  u32 t8v = t8;
  PIN(t8v);
  SCHED();
  if (LBAR & 4) asm volatile("s_barrier" ::: "memory");
  __builtin_amdgcn_sched_barrier(0);
  const int rot = ROT * b;
  auto slot = [&](int c) { return (c & ~7) | ((c - rot) & 7); };
  auto load_wo = [&](int r0, int r1) {
    const uint16_t* p = wo + (size_t)(go * 512 + (((l >> 3) & 1) * 4 + kg) * 64 + w) * 512 + s * 64 + (l & 7) * 8;
#pragma unroll
    for (int r = r0; r < r1; ++r) wor[r] = *(const u32x4*)(p + ((r + rot) & 7) * 8 * 512);
  };
  auto load_w1 = [&](int r0, int r1) {
    const uint16_t* p = w1 + (size_t)(w * 64 + ((l >> 3) & 1) * 32 + kg * 8) * FF + f0 + (l & 7) * 4;
#pragma unroll
    for (int r = r0; r < r1; ++r) w1r[r] = *(const u32x2*)(p + ((r + rot) & 7) * FF);
  };
  auto load_w2 = [&](int r0, int r1) {
    const uint16_t* p2 = w2 + (size_t)(f0 + kg * 8) * 512 + w * 64 + i16 * 4;
#pragma unroll
    for (int r = r0; r < r1; ++r) w2r[r] = *(const u32x2*)(p2 + ((r + rot) & 7) * 512);
  };

  const int xo = X2_OFF + bg * 2048 + w * 256 + c8 * 4;
  u32x4 vt[8], wot[8], w1t[4], w2t[4];
  float csq = 0.f, cs1 = 0.f;
  auto red8 = [&](const float (&v)[8]) {
    const float u0 = rs32(v[0], v[4]), u1 = rs32(v[1], v[5]), u2 = rs32(v[2], v[6]), u3 = rs32(v[3], v[7]);
    return rs8(rs16(u0, u2), rs16(u1, u3), bit3);
  };

#pragma unroll
  for (int layer = 0; layer < 2; ++layer) {
    {
      const int qo = (bg * 8 + (l >> 3)) * 2048 + w * 256 + (l & 7) * 16;
      int od = OP_OFF + ((layer * 32 + bg) * 8 + s) * 2048 + w * 256 + ((slot(l) >> 2) & 1) * 128 + (l >> 3) * 16 + (slot(l) & 3) * 4;
      PIN(od);
      float a[8], mean = 0.f, rstd = 1.f;
      if (layer == 1) {
        u32x4 v[2];
        const int off[2] = {CS_OFF + qo, CS_OFF + qo + 128};
        gather<2>(rs, off, t8, v, none);
        float c[8];
#pragma unroll
        for (int j = 0; j < 4; ++j) { c[j] = asf(v[0][j]); c[4 + j] = asf(v[1][j]); }
        csq = red8(c);
        PIN(csq);
      }
      DLY(layer * 4);
      if (layer == 0) {
        u32x4 v[2];
        const int off[2] = {QP_OFF + qo, QP_OFF + qo + 128};
        gather<2>(rs, off, t8, v, [&]() {
          load_v(VSPLIT, 8);
          SCHED();
          if (WO_A > 0) asm volatile("s_barrier" ::: "memory");
          SCHED();
          load_wo(0, WO_A);
        });
        STAMP(2);
#pragma unroll
        for (int j = 0; j < 4; ++j) { a[j] = asf(v[0][j]); a[4 + j] = asf(v[1][j]); }
      } else {
        u32x4 v[3];
        const int off[3] = {ST_OFF + (bg * 8 + (l >> 3)) * 128 + (l & 7) * 16, QP_OFF + 256 * 2048 + qo, QP_OFF + 256 * 2048 + qo + 128};
        gather<3>(rs, off, t8, v, [&]() { xres = __builtin_amdgcn_raw_buffer_load_b32(rs, xo, 0, AUX_SC1); });
        STAMP(10);
        CST(0);
#pragma unroll
        for (int j = 0; j < 4; ++j) { a[j] = asf(v[1][j]); a[4 + j] = asf(v[2][j]); }
        float u = rs32(asf(v[0][0]) + asf(v[0][1]), asf(v[0][2]) + asf(v[0][3]));
        u = rs16(u, u);
        u += dpp<ROR8>(u);
        auto r = __builtin_amdgcn_permlane32_swap(asu(u), asu(u), false, false);
        mean = asf(r[0]) * (1.f / 512.f);
        rstd = __builtin_amdgcn_rsqf(fmx(asf(r[1]) * (1.f / 512.f) - mean * mean, 0.f) + EPS);
      }
      const float qv = layer == 0 ? red8(a) : (red8(a) - mean * csq) * rstd;
      s_q[w * 64 + c8] = (uint16_t)(pk_bf16(qv, qv) & 0xffffu);
      CST(1);
      wg_barrier();
      CST(2);
      const u32x4 qf = *(const u32x4*)(s_q + (i16 >> 2) * 128 + kg * 32 + (i16 & 3) * 8);
      f32x4 d[8];
#pragma unroll
      for (int m = 0; m < 8; ++m) d[m] = mfma(kr[m], qf, zero4);
      SCHED();
      float sv[8];
#pragma unroll
      for (int m = 0; m < 8; ++m) { sv[m] = d[m][0] + dpp<0x55>(d[m][1]); PIN(sv[m]); }
#pragma unroll
      for (int m = 0; m < 8; ++m) { sv[m] += dpp<0xAA>(d[m][2]); PIN(sv[m]); }
#pragma unroll
      for (int m = 0; m < 8; ++m) { sv[m] += dpp<0xFF>(d[m][3]); PIN(sv[m]); }
      CST(3);
      if (layer == 0) {
#pragma unroll
        for (int j = 0; j < 4; ++j) {
          const u32 c = pk_bf16(sv[2 * j], sv[2 * j + 1]);
          const f32x2 t = f32x2{asf(c << 16), asf(c & 0xffff0000u)} * 0.08838834764831845f;
          const u32 e = pk_bf16(t[0], t[1]);
          sv[2 * j] = asf(e << 16); sv[2 * j + 1] = asf(e & 0xffff0000u);
        }
      }
      const float sc = (layer == 0 ? 1.f : 0.08838834764831845f) * 1.4426950408889634f;
      float m = fmx(fmx(fmx(fmx(sv[0], sv[1]), sv[2]), fmx(fmx(sv[3], sv[4]), sv[5])), fmx(sv[6], sv[7]));
      m = mx32(mx16(m)) * sc;
      u32x4 pa;
#pragma unroll
      for (int j = 0; j < 4; ++j) {
        const f32x2 e = f32x2{sv[2 * j], sv[2 * j + 1]} * sc - m;
        pa[j] = pk_bf16(__builtin_amdgcn_exp2f(e[0]), __builtin_amdgcn_exp2f(e[1]));
      }
      CST(4);
      if (layer == 0) { SCHED(); load_wo(WO_A, WO_B); load_w1(0, W_E); load_w2(0, W_E); SCHED(); tr8<4>(vr, vt); }
      f32x4 dl = mfma(pa, ones, zero4);
      SCHED();
#pragma unroll
      for (int c2 = 0; c2 < 8; ++c2) d[c2] = mfma(pa, vt[c2], zero4);
      PIN(dl);
#pragma unroll
      for (int c2 = 0; c2 < 8; ++c2) PIN(d[c2]);
      if (i16 == 4 * kg) { s_ml[kg * 16 + w] = m; s_ml[kg * 16 + 8 + w] = dl[0]; }
#pragma unroll
      for (int c2 = 0; c2 < 8; ++c2) s_ow[(w * 8 + c2) * 64 + l] = d[c2][0];
      CST(5);
      wg_barrier();
      CST(6);
      const f32x4 m0 = *(const f32x4*)(s_ml + kg * 16), m1 = *(const f32x4*)(s_ml + kg * 16 + 4);
      const f32x4 l0 = *(const f32x4*)(s_ml + kg * 16 + 8), l1 = *(const f32x4*)(s_ml + kg * 16 + 12);
      float ov[8];
#pragma unroll
      for (int u = 0; u < 8; ++u) ov[u] = s_ow[(u * 8 + w) * 64 + l];
      const float M = fmx(fmx(fmx(fmx(m0[0], m0[1]), m0[2]), fmx(fmx(m0[3], m1[0]), m1[1])), fmx(m1[2], m1[3]));
      const f32x2 mp[4] = {lo2(m0), hi2(m0), lo2(m1), hi2(m1)}, lp[4] = {lo2(l0), hi2(l0), lo2(l1), hi2(l1)};
      f32x2 L2, o2;
#pragma unroll
      for (int u = 0; u < 4; ++u) {
        const f32x2 e = mp[u] - M;
        const f32x2 c = {__builtin_amdgcn_exp2f(e[0]), __builtin_amdgcn_exp2f(e[1])};
        const f32x2 ou = {ov[2 * u], ov[2 * u + 1]};
        L2 = u ? c * lp[u] + L2 : c * lp[u];
        o2 = u ? c * ou + o2 : c * ou;
      }
      PIN(L2); PIN(o2);
      const float L = L2[0] + L2[1];
      const float on = (o2[0] + o2[1]) * __builtin_amdgcn_rcpf(L);
      const float lw = M + __builtin_amdgcn_logf(L);
      __builtin_amdgcn_raw_buffer_store_b32(__builtin_amdgcn_perm(pk_bf16(on, on), asu(lw), 0x0706000cu | ((slot(l) & 3) << 8)) | t8, rs, od, 0, AUX_SC1);
      STAMP(3 + layer * 8);
      CST(7);
    }
    {
      if (layer == 0) { SCHED(); load_wo(WO_B, 8); SCHED(); }
      const int oo = OP_OFF + ((layer * 32 + b * 8 + go) * 8 + (l & 7)) * 2048 + w * 256 + (((l >> 3) & 1) * 4 + kg) * 16;
      const int off[2] = {oo, oo + 128};
      int wd = WP_OFF + (layer * 4 + b) * 16384 + g * 2048 + s * 256 + (l & 1) * 128 + (i16 >> 1) * 16 + (l >> 5) * 8 + (kg & 1) * 4;
      PIN(wd);
      u32x4 v[2];
      DLY(1 + layer * 4);
      if (layer == 0) gather<2, false>(rs, off, t8, v, [&]() {
        if (W1POS == 0) { load_w1(W_E, 8); SCHED(); }
        tr8<4>(wor, wot);
#pragma unroll
        for (int c = 0; c < 8; ++c) PIN(wot[c]);
      }); else gather<2, false>(rs, off, t8, v, [&]() {
        if (__builtin_expect(!__all(((xres ^ t16) & 0xffffu) == 0), 0)) {
          for (u32 spins = 0; spins < SPIN_CAP; ++spins) {
            asm volatile("" ::: "memory");
            xres = __builtin_amdgcn_raw_buffer_load_b32(rs, xo, 0, AUX_SC1);
            if (__all(((xres ^ t16) & 0xffffu) == 0)) break;
          }
        }
        xres &= 0xffff0000u;
        PIN(xres);
      });
      STAMP(4 + layer * 8);
      CST(8);
      if (layer == 0 && W1POS == 2) { SCHED(); load_w1(W_E, 8); SCHED(); }
      const float lw = asf(__builtin_amdgcn_perm(__builtin_amdgcn_perm(v[0][3], v[0][2], 0x05010000u), __builtin_amdgcn_perm(v[0][1], v[0][0], 0x00000501u), 0x07060100u));
      float M = fmx(lw, dpp<QX1>(lw)); M = fmx(M, dpp<QX2>(M)); M = fmx(M, dpp<HMIR>(M));
      float cw = __builtin_amdgcn_exp2f(lw - M);
      float T = cw + dpp<QX1>(cw); T += dpp<QX2>(T); T += dpp<HMIR>(T);
      cw *= __builtin_amdgcn_rcpf(T);
      u32x4 a;
#pragma unroll
      for (int j = 0; j < 4; ++j)
        a[j] = pk_bf16(asf(v[j >> 1][(j & 1) * 2] & 0xffff0000u) * cw, asf(v[j >> 1][(j & 1) * 2 + 1] & 0xffff0000u) * cw);
      f32x4 d[8];
#pragma unroll
      for (int c = 0; c < 8; ++c) d[c] = mfma(a, wot[c], zero4);
      f32x4 z[2];
#pragma unroll
      for (int c = 0; c < 8; ++c) { f32x2 t = lo2(d[c]) + hi2(d[c]); PIN(t); float r = t[0] + t[1]; PIN(r); z[c >> 2][c & 3] = r; }
      if (((l >> 3) & 1) == (l >> 5)) {
        *(f32x4*)(s_wp + (w * 4 + kg) * 64 + (l & 7) * 8) = z[0];
        *(f32x4*)(s_wp + (w * 4 + kg) * 64 + (l & 7) * 8 + 4) = z[1];
      }
      if (layer == 0 && W1POS == 3) { SCHED(); load_w1(W_E, 8); SCHED(); }
      CST(9);
      wg_barrier();
      CST(10);
      if (w == 0) {
        f32x4 p[8];
#pragma unroll
        for (int k = 0; k < 8; ++k) p[k] = *(const f32x4*)(s_wp + (kg * 8 + k) * 64 + i16 * 4);
        const f32x4 acc = ((p[0] + p[1]) + (p[2] + p[3])) + ((p[4] + p[5]) + (p[6] + p[7]));
        const float t = rs16(rs32(acc[0], acc[2]), rs32(acc[1], acc[3]));
        __builtin_amdgcn_raw_buffer_store_b32(tag8(t, t8v), rs, wd, 0, AUX_SC1);
      }
      STAMP(5 + layer * 8);
      CST(11);
    }
    {
      if (layer == 0 && W1POS == 1) { SCHED(); load_w1(W_E, 8); SCHED(); }
      if (layer == 0 && W2POS == 1) { SCHED(); load_w2(W_E, 8); SCHED(); }
      if (layer == 0 && WQPOS == 2) { SCHED(); load_wq(); SCHED(); }
      const int wo_ = WP_OFF + (layer * 4 + b) * 16384 + (l >> 3) * 2048 + w * 256 + (l & 7) * 16;
      const int off[2] = {wo_, wo_ + 128};
      int md = MP_OFF + (((layer * 4 + b) * 8 + w) * 56 + g * 7 + s) * 256 + l * 16;
      PIN(md);
      u32x4 v[2];
      DLY(2 + layer * 4);
      if (layer == 0) gather<2>(rs, off, t8, v, [&]() {
        if (W2POS == 0) load_w2(W_E, 8);
        if (WQPOS == 1) load_wq();
        SCHED();
        tr8<2>(w1r, w1t);
#pragma unroll
        for (int c = 0; c < 4; ++c) PIN(w1t[c]);
      }); else gather<2>(rs, off, t8, v, none);
      STAMP(6 + layer * 8);
      if (layer == 0 && WQPOS == 0) { SCHED(); load_wq(); SCHED(); }
      CST(12);
      float y[8];
#pragma unroll
      for (int j = 0; j < 8; ++j) y[j] = asf(v[j >> 2][j & 3]);
      const float x1 = rbf(red8(y)) + asf(xres);
      s_x1[w * 64 + slot(c8)] = (uint16_t)(pk_bf16(x1, x1) & 0xffffu);
      asm volatile("" ::: "memory");
      const int xf = w * 64 + (i16 & 1) * 32 + kg * 8;
      const u32x4 am = *(const u32x4*)(s_x1 + (bit3 ? 512 : xf));
      const u32x4 bx = *(const u32x4*)(s_x1 + xf);
      f32x4 d[4];
#pragma unroll
      for (int c = 0; c < 4; ++c) d[c] = mfma(am, w1t[c], zero4);
      f32x4 gr = mfma(am, bx, zero4);
#pragma unroll
      for (int c = 0; c < 4; ++c) PIN(d[c]);
      if (l < 16 || (layer == 0 && kg == 2)) {
        const f32x4 hv = bit3 ? f32x4{d[0][1], d[1][1], d[2][1], d[3][1]} : f32x4{d[0][0], d[1][0], d[2][0], d[3][0]};
        *(f32x4*)(s_hp + (l >> 5) * 512 + (w * 2 + ((l >> 3) & 1)) * 32 + (l & 7) * 4) = hv;
      }
      if ((l & 30) == 0) s_st[(l >> 5) * 16 + w * 2 + (l & 1)] = (l & 1) ? gr[1] : gr[0];
      if (layer == 0) tr8<2>(w2r, w2t);
      wg_barrier();
      if (mlp_on) {
        float st = s_st[l & 31];
        float hp8[8];
#pragma unroll
        for (int p = 0; p < 8; ++p) hp8[p] = s_hp[((l >> 5) * 8 + p) * 32 + (l & 31)];
        if (layer == 0) {
          float ac = 0.f;
#pragma unroll
          for (int p = 0; p < 8; ++p) ac += s_hp[512 + ((l >> 5) * 8 + p) * 32 + (l & 31)];
          cs1 = rs32(ac, ac);
        }
        st += dpp<ROR8>(st); st += dpp<ROR4>(st); st += dpp<ROR2>(st); st += dpp<ROR1>(st);
        float acc = sum8(hp8);
        acc = rs32(acc, acc);
        auto r = __builtin_amdgcn_permlane16_swap(asu(st), asu(st), false, false);
        const float mu = asf(r[1]) * (1.f / 512.f);
        const float var = fmx(asf(r[0]) * (1.f / 512.f) - mu * mu, 0.f);
        const float r12 = __builtin_amdgcn_rsqf(var + EPS * (var + EPS));
        const float hp = (acc - mu * cs1) * r12 + b1v;
        const float u = hp * (-2.302208198144325f - 0.1029432395800235f * (hp * hp));
        const float hv = hp * __builtin_amdgcn_rcpf(1.f + __builtin_amdgcn_exp2f(u));
        s_a[w * 64 + slot(l)] = (uint16_t)(pk_bf16(hv, hv) & 0xffffu);
        asm volatile("" ::: "memory");
        const u32x4 ha = *(const u32x4*)(s_a + w * 64 + kg * 8);
        f32x4 e[4];
#pragma unroll
        for (int c = 0; c < 4; ++c) e[c] = mfma(ha, w2t[c], zero4);
#pragma unroll
        for (int c = 0; c < 4; ++c) PIN(e[c]);
        const u32x4 o = {tag8(e[0][0], t8v), tag8(e[1][0], t8v), tag8(e[2][0], t8v), tag8(e[3][0], t8v)};
        if (l < 16) stsc1(rs, md, o);
      }
      STAMP(7 + layer * 8);
      CST(13);
    }
    if (layer == 0) {
      float xr = asf((u32)s_x1[s * 64 + slot(l)] << 16);
      PIN(xr);
      const int mo = MP_OFF + ((b * 8 + s) * 56 + w * 7 + kg) * 256 + i16 * 16;
      const int off[2] = {mo, kg < 3 ? mo + 1024 : mo};
      float two = kg < 3 ? 1.f : 0.f;
      int qd = QP_OFF + ((32 + b * 8 + gq) * 8 + s) * 2048 + w * 256 + i16 * 16;
      int sd = ST_OFF + ((b * 8 + gq) * 8 + s) * 128 + (l & 7) * 16 + (l >> 5) * 8;
      int xpi = w * 64 + i16 * 4 + (l >> 5) * 2 + (kg & 1), sai = w * 64 + (i16 & 1) * 32 + kg * 8;
      float b2p = b2a;
      PIN(two); PIN(qd); PIN(sd); PIN(xpi); PIN(sai); PIN(b2p);
      u32x4 v[2];
      DLY(3);
      gather<2>(rs, off, t8, v, [&]() {
        tr8<4>(wqr, wqt);
#pragma unroll
        for (int c = 0; c < 8; ++c) PIN(wqt[c]);
      });
      STAMP(8);
      float y[8];
#pragma unroll
      for (int j = 0; j < 4; ++j) y[j] = asf(v[0][j]) + two * asf(v[1][j]);
      s_xp[xpi] = rs16(rs32(y[0], y[2]), rs32(y[1], y[3]));
      wg_barrier();
#pragma unroll
      for (int u = 0; u < 8; ++u) y[u] = s_xp[u * 64 + l];
      const float x2f = rbf(rbf(sum8(y)) + b2p) + xr;
      const u32 x2 = pk_bf16(x2f, x2f);
      s_a[w * 64 + l] = (uint16_t)(x2 & 0xffffu);
      asm volatile("" ::: "memory");
      const u32x4 a = *(const u32x4*)(s_a + sai);
      if (w == 6) {
        const float xv = asf(x2 & 0xffff0000u);
        float u = rs32(xv, xv * xv);
        u = rs16(u, u);
        u += dpp<ROR8>(u); u += dpp<ROR4>(u); u += dpp<ROR2>(u); u += dpp<ROR1>(u);
        const float hi = asf(asu(u) & 0xffffff00u);
        const u32x2 o = {asu(hi) | t8, tag8(u - hi, t8v)};
        if ((l & 24) == 0) __builtin_amdgcn_raw_buffer_store_b64(o, rs, sd, 0, AUX_SC1);
      }
      f32x4 d[8];
      s1_mm(a, d);
      s1_out(d, 0, t8v, qd, l < 16);
      if (w == 7) __builtin_amdgcn_raw_buffer_store_b32((x2 & 0xffff0000u) | t16, rs, X2_OFF + (b * 8 + gq) * 2048 + s * 256 + l * 4, 0, AUX_SC1);
      STAMP(9);
    } else if (w == 0) {
      const int oc = s * 64 + g * 8 + e8;
      const float xr = asf((u32)s_x1[slot(oc)] << 16);
      const int mo = MP_OFF + (((4 + b) * 8 + s) * 56 + (l < 56 ? l : 0)) * 256 + g * 32;
      const int off[2] = {mo, mo + 16};
      u32x4 v[2];
      DLY(7);
      gather<2>(rs, off, t8, v, none);
      STAMP(16);
      float y[8];
#pragma unroll
      for (int j = 0; j < 8; ++j) y[j] = l < 56 ? asf(v[j >> 2][j & 3]) : 0.f;
      float t = red8(y);
      t += dpp<QX1>(t); t += dpp<QX2>(t); t += dpp<HMIR>(t);
      const float r = rbf(rbf(t) + b2f) + xr;
      if ((l & 7) == 0) out[b * 512 + oc] = (uint16_t)(pk_bf16(r, r) & 0xffffu);
      if (l == 0 && g == 0 && s == 0) __builtin_amdgcn_raw_buffer_store_b32(tn, rs, EP_OFF + b * 256, 0, AUX_SC1);
      STAMP(17);
    }
  }
}

extern "C" void kernel_launch(void* const* d_in, const int* in_sizes, int n_in, void* d_out, int out_size, void* d_ws,
                              size_t ws_size, hipStream_t stream) {
  (void)in_sizes; (void)n_in; (void)out_size; (void)ws_size;
  fused_decode<<<256, 512, 0, stream>>>((const uint16_t*)d_in[0], (const uint16_t*)d_in[1], (const uint16_t*)d_in[2],
                                        (const uint16_t*)d_in[3], (const uint16_t*)d_in[4], (const uint16_t*)d_in[6],
                                        (const uint16_t*)d_in[7], (const uint16_t*)d_in[8], (const uint16_t*)d_in[9],
                                        (uint16_t*)d_out, (char*)d_ws TUNE_ARG);
}
```
